```python
import torch

def _softmax(x, axis=-1):
    return torch.softmax(x, dim=axis)
def _gelu(x):
    return torch.nn.functional.gelu(x, approximate="tanh")
def _silu(x):
    return torch.nn.functional.silu(x)
def _sigmoid(x):
    return torch.sigmoid(x)
def _layernorm(x, axis=-1, eps=1e-5):
    m = x.mean(dim=axis, keepdim=True)
    v = x.var(dim=axis, keepdim=True, unbiased=False)
    return (x - m) / torch.sqrt(v + eps)
def _scatter_add(tgt, idx, src, axis=0):
    return tgt.clone().index_add_(axis, idx, src)
def _segment_sum(src, seg, n):
    out = torch.zeros((n,) + src.shape[1:], dtype=src.dtype)
    return out.index_add_(0, seg, src)
def _pad(x, pw):
    flat = tuple(v for pair in reversed(pw) for v in pair)
    return torch.nn.functional.pad(x, flat)

kernel_name = 'parallel_sum__ssm_conv_s4__swiglu_ffn__prompt'

def setup_inputs():

    _w = (lambda a: (lambda t: t.to(torch.bfloat16) if t.is_floating_point() else t)(torch.as_tensor(a)))
    def _A_setup():
        B, L, D, N = 1, 2048, 512, 128
        A_re = -(lambda _x: torch.exp(torch.as_tensor(_x)))((lambda *s: torch.randn(*s, dtype=torch.bfloat16) * 3.2)(D, N))
        A_im = torch.pi * (lambda *s: torch.randn(*s, dtype=torch.bfloat16) * 0.1)(D, N)
        B_re = (lambda *s: torch.randn(*s, dtype=torch.bfloat16) * 0.4)(D, N)
        B_im = (lambda *s: torch.randn(*s, dtype=torch.bfloat16) * 0.4)(D, N)
        C_re = (lambda *s: torch.randn(*s, dtype=torch.bfloat16) * 0.4)(D, N)
        C_im = (lambda *s: torch.randn(*s, dtype=torch.bfloat16) * 0.4)(D, N)
        log_dt = (lambda *s: torch.randn(*s, dtype=torch.bfloat16) * 0.1)(D) * 0.5 - 4.0
        Dskip = (lambda *s: torch.randn(*s, dtype=torch.bfloat16) * 0.2)(D)
        return {
            "u": _w((lambda *s: torch.randn(*s, dtype=torch.bfloat16) * 0.05)(B, L, D)),
            "A_re": _w(A_re),
            "A_im": _w(A_im),
            "B_re": _w(B_re),
            "B_im": _w(B_im),
            "C_re": _w(C_re),
            "C_im": _w(C_im),
            "log_dt": _w(log_dt),
            "Dskip": _w(Dskip),
        }
    _A_in = _A_setup()

    def _B_setup():
        B, S, D, F = 1, 2048, 512, 1376
        return {
            "x": _w((lambda *s: torch.randn(*s, dtype=torch.bfloat16) * 0.05)(B, S, D)),
            "w_gate": _w((lambda *s: torch.randn(*s, dtype=torch.bfloat16) * 0.05)(D, F)),
            "w_up": _w((lambda *s: torch.randn(*s, dtype=torch.bfloat16) * 0.05)(D, F)),
            "w_down": _w((lambda *s: torch.randn(*s, dtype=torch.bfloat16) * 0.05)(F, D)),
        }
    _B_in = _B_setup()

    return {
        "A_u": _A_in["u"],
        "A_A_re": _A_in["A_re"],
        "A_A_im": _A_in["A_im"],
        "A_B_re": _A_in["B_re"],
        "A_B_im": _A_in["B_im"],
        "A_C_re": _A_in["C_re"],
        "A_C_im": _A_in["C_im"],
        "A_log_dt": _A_in["log_dt"],
        "A_Dskip": _A_in["Dskip"],
        "B_x": _B_in["x"],
        "B_w_gate": _B_in["w_gate"],
        "B_w_up": _B_in["w_up"],
        "B_w_down": _B_in["w_down"],
    }

def reference(A_u, A_A_re, A_A_im, A_B_re, A_B_im, A_C_re, A_C_im, A_log_dt, A_Dskip, B_x, B_w_gate, B_w_up, B_w_down):
    def _A_ref(u, A_re, A_im, B_re, B_im, C_re, C_im, log_dt, Dskip):
        Bsz, L, D = u.shape
        dt = torch.exp(log_dt)[:, None]
        A = A_re + 1j * A_im
        Bc = B_re + 1j * B_im
        Cc = C_re + 1j * C_im
        logdA = dt * A
        dB = dt * Bc
        l = (lambda x, dt: x.to(dt))(torch.arange(L), A_re.dtype)
        P = torch.exp(logdA[:, :, None] * l[None, None, :])
        K = torch.einsum("dn,dnl,dn->dl", Cc, P, dB).real
        L2 = 2 * L
        Kp = _pad(K, ((0, 0), (0, L)))
        up = (lambda x, *d: x.permute(*d))(u, 0, 2, 1)
        up = _pad(up, ((0, 0), (0, 0), (0, L)))
        Kf = (lambda x, axes=(-2,-1): torch.fft.rfft2(x.float(), dim=axes))(Kp[:, None, :], axes=(-2, -1))
        Uf = (lambda x, axes=(-2,-1): torch.fft.rfft2(x.float(), dim=axes))(up[:, :, None, :], axes=(-2, -1))
        Yf = Uf * Kf[None, :, :, :]
        y = (lambda x, s, axes=(-2,-1): torch.fft.irfft2(x, s=s, dim=axes).to(torch.bfloat16))(Yf, (1, L2), axes=(-2, -1))[:, :, 0, :L]
        y = (lambda x, *d: x.permute(*d))(y, 0, 2, 1) + u * Dskip[None, None, :]
        return y

    def _B_ref(x, w_gate, w_up, w_down):
        g = _silu(x @ w_gate)
        u = x @ w_up
        return (g * u) @ w_down

    x = A_u
    xn = _layernorm(x, axis=-1)
    return x + _A_ref(xn, A_A_re, A_A_im, A_B_re, A_B_im, A_C_re, A_C_im, A_log_dt, A_Dskip) + _B_ref(xn, B_w_gate, B_w_up, B_w_down)
```

```cpp
#include <hip/hip_runtime.h>
#include <hip/hip_bf16.h>
#include <stdint.h>
#define STAIR 1
typedef unsigned short u16;
constexpr int L = 2048, D = 512, NS = 128, F = 1376;
__device__ __forceinline__ float bf2f(u16 v) { return __uint_as_float(((unsigned)v) << 16); }
__device__ __forceinline__ u16 f2bf(float f) { __hip_bfloat16 b = __float2bfloat16(f); return *(u16*)&b; }
__device__ __forceinline__ float rbf(float f) { return bf2f(f2bf(f)); }

__global__ void k_ln(const u16* x, u16* xn) {
  int t = blockIdx.x, lane = threadIdx.x;
  float v[8]; float s = 0;
  for (int i = 0; i < 8; i++) { v[i] = bf2f(x[t * D + lane * 8 + i]); s += v[i]; }
  for (int o = 32; o; o >>= 1) s += __shfl_xor(s, o);
  float m = s / D; float q = 0;
  for (int i = 0; i < 8; i++) q += (v[i] - m) * (v[i] - m);
  for (int o = 32; o; o >>= 1) q += __shfl_xor(q, o);
  float mb = rbf(m), vb = rbf(q / D);
  float sd = rbf(sqrtf(rbf(vb + 1e-5f)));
  for (int i = 0; i < 8; i++) xn[t * D + lane * 8 + i] = f2bf(rbf(v[i] - mb) / sd);
}
__global__ void k_kgen(const u16* Are, const u16* Aim, const u16* Bre, const u16* Bim, const u16* Cre, const u16* Cim, const u16* ldt, float* K) {
  int d = blockIdx.y, l = blockIdx.x * 256 + threadIdx.x;
  float dt = rbf(expf(bf2f(ldt[d])));
  float lf = STAIR ? rbf((float)l) : (float)l;
  float acc = 0;
  for (int n = 0; n < NS; n++) {
    int i = d * NS + n;
    float ar = dt * bf2f(Are[i]), ai = dt * bf2f(Aim[i]);
    float br = dt * bf2f(Bre[i]), bi = dt * bf2f(Bim[i]);
    float cr = bf2f(Cre[i]), ci = bf2f(Cim[i]);
    float e = expf(ar * lf); float s, c; sincosf(ai * lf, &s, &c);
    float pr = e * c, pi = e * s;
    float wr = cr * br - ci * bi, wi = cr * bi + ci * br;
    acc += wr * pr - wi * pi;
  }
  K[d * L + l] = acc;
}
__global__ void k_conv(const u16* xn, const float* K, float* y) {
  int d = blockIdx.x * 64 + (threadIdx.x & 63), t = blockIdx.y * 4 + (threadIdx.x >> 6);
  float acc = 0;
  for (int l = 0; l <= t; l++) acc += K[d * L + l] * bf2f(xn[(t - l) * D + d]);
  y[t * D + d] = acc;
}
__global__ void k_g1(const u16* xn, const u16* wg, const u16* wu, u16* h) {
  int f = blockIdx.x * 64 + (threadIdx.x & 63), t = blockIdx.y * 4 + (threadIdx.x >> 6);
  if (f >= F) return;
  float g = 0, u = 0;
  for (int k = 0; k < D; k++) { float a = bf2f(xn[t * D + k]); g += a * bf2f(wg[k * F + f]); u += a * bf2f(wu[k * F + f]); }
  g = rbf(g); u = rbf(u);
  float s = rbf(g / (1.f + expf(-g)));
  h[t * F + f] = f2bf(s * u);
}
__global__ void k_g2(const u16* h, const u16* wd, const u16* x, const u16* xn, const float* y, const u16* dsk, u16* out) {
  int d = blockIdx.x * 64 + (threadIdx.x & 63), t = blockIdx.y * 4 + (threadIdx.x >> 6);
  float a = 0;
  for (int k = 0; k < F; k++) a += bf2f(h[t * F + k]) * bf2f(wd[k * D + d]);
  a = rbf(a);
  float r = rbf(rbf(y[t * D + d]) + rbf(bf2f(xn[t * D + d]) * bf2f(dsk[d])));
  r = rbf(bf2f(x[t * D + d]) + r);
  out[t * D + d] = f2bf(r + a);
}
extern "C" void kernel_launch(void* const* d_in, const int* in_sizes, int n_in, void* d_out, int out_size, void* d_ws, size_t ws_size, hipStream_t stream) {
  const u16* x = (const u16*)d_in[0];
  char* ws = (char*)d_ws;
  u16* xn = (u16*)ws; float* K = (float*)(ws + (4 << 20)); float* y = (float*)(ws + (8 << 20)); u16* h = (u16*)(ws + (16 << 20));
  k_ln<<<L, 64, 0, stream>>>(x, xn);
  k_kgen<<<dim3(L / 256, D), 256, 0, stream>>>((const u16*)d_in[1], (const u16*)d_in[2], (const u16*)d_in[3], (const u16*)d_in[4], (const u16*)d_in[5], (const u16*)d_in[6], (const u16*)d_in[7], K);
  k_conv<<<dim3(D / 64, L / 4), 256, 0, stream>>>(xn, K, y);
  k_g1<<<dim3((F + 63) / 64, L / 4), 256, 0, stream>>>(xn, (const u16*)d_in[10], (const u16*)d_in[11], h);
  k_g2<<<dim3(D / 64, L / 4), 256, 0, stream>>>(h, (const u16*)d_in[12], x, xn, y, (const u16*)d_in[8], (u16*)d_out);
}
```

```cpp
#include <hip/hip_runtime.h>
#include <hip/hip_bf16.h>
#include <stdint.h>

typedef unsigned short u16;
typedef unsigned int u32;
typedef __attribute__((ext_vector_type(8))) short bf16x8;
typedef __attribute__((ext_vector_type(4))) short bf16x4;
typedef __attribute__((ext_vector_type(4))) float f32x4;
typedef __attribute__((ext_vector_type(16))) float f32x16;
typedef __attribute__((ext_vector_type(4))) u32 u32x4;
typedef __attribute__((ext_vector_type(2))) u32 u32x2;
typedef __attribute__((ext_vector_type(2))) float f32x2;
typedef __attribute__((ext_vector_type(2))) __bf16 bf2_t;
#define GAS __attribute__((address_space(1)))
#define LAS __attribute__((address_space(3)))
#define DEVI __device__ __forceinline__

constexpr int L = 2048, D = 512, NS = 128, F = 1376;
constexpr int NFT = 43;
constexpr int KS2 = 86;
constexpr int KS2P = 88;
constexpr size_t OFF_XF = 0;
constexpr size_t OFF_XT = 2u << 20;
constexpr size_t OFF_W1 = 4u << 20;
constexpr size_t OFF_W2 = 8u << 20;
constexpr size_t OFF_KR = 10u << 20;
constexpr size_t OFF_HF = 16u << 20;
constexpr size_t OFF_YT = 24u << 20;
constexpr size_t OFF_FL = 28u << 20;

DEVI float bflo(u32 v) { return __uint_as_float(v << 16); }
DEVI float bfhi(u32 v) { return __uint_as_float(v & 0xffff0000u); }
DEVI float bf1(u16 v) { return __uint_as_float(((u32)v) << 16); }
DEVI u32 pk2(float lo, float hi) { f32x2 f = {lo, hi}; bf2_t b = __builtin_convertvector(f, bf2_t); return __builtin_bit_cast(u32, b); }
DEVI float rbf(float f) { return bflo(pk2(f, f)); }
DEVI void glds16(const void* g, void* l) { __builtin_amdgcn_global_load_lds((const GAS u32*)g, (LAS u32*)l, 16, 0, 0); }
#define WAITV(n) asm volatile("s_waitcnt vmcnt(" #n ")" ::: "memory")
#define WAITL0() asm volatile("s_waitcnt lgkmcnt(0)" ::: "memory")
#define BAR() __builtin_amdgcn_s_barrier()

struct cf { float r, i; };
DEVI cf cmul(cf a, cf b) { cf c; c.r = a.r * b.r - a.i * b.i; c.i = a.r * b.i + a.i * b.r; return c; }
DEVI cf cexp_(float ar, float ai, float x) {
  float e = __builtin_amdgcn_exp2f(ar * x * 1.44269504088896341f);
  float rev = __builtin_amdgcn_fractf(ai * x * 0.15915494309189535f);
  cf c; c.r = e * __builtin_amdgcn_cosf(rev); c.i = e * __builtin_amdgcn_sinf(rev); return c;
}

constexpr int KG_ROW = 272;
constexpr int KG_WAVE = (32 + 32) * KG_ROW;
constexpr int K0_LDS = 4 * KG_WAVE + 2 * 4096 + 2 * 1408;

__global__ __launch_bounds__(256) void k_prep(const u16* __restrict__ x, const u16* __restrict__ Are, const u16* __restrict__ Aim,
    const u16* __restrict__ Bre, const u16* __restrict__ Bim, const u16* __restrict__ Cre, const u16* __restrict__ Cim,
    const u16* __restrict__ ldt, const u16* __restrict__ wg, const u16* __restrict__ wu, const u16* __restrict__ wd, char* __restrict__ ws) {
  __shared__ __attribute__((aligned(16))) char sm[K0_LDS];
  const int b = blockIdx.x, tid = threadIdx.x, lane = tid & 63, w = tid >> 6;
  if (b < 256) {
    if (tid == 0) *(u32*)(ws + OFF_FL + (size_t)b * 128) = 0u;
    const int dd = w >> 1, d = 2 * b + dd, n = (w & 1) * 64 + lane, gi = d * NS + n;
    const float dt = rbf(__expf(bf1(ldt[d])));
    const float ar = dt * bf1(Are[gi]), ai = dt * bf1(Aim[gi]);
    cf Bc = {dt * bf1(Bre[gi]), dt * bf1(Bim[gi])}, Cc = {bf1(Cre[gi]), bf1(Cim[gi])};
    char* tx = sm + w * KG_WAVE; char* ty = tx + 32 * KG_ROW;
    auto putx = [&](int r, cf v) { *(u32*)(tx + r * KG_ROW + lane * 4) = pk2(v.r, -v.i); };
    auto puty = [&](int r, cf v) { *(u32*)(ty + r * KG_ROW + lane * 4) = pk2(v.r, v.i); };
    cf X[21];
    X[0] = cmul(Cc, Bc);
    cf e1 = cexp_(ar, ai, 1.f), e2 = cmul(e1, e1), e4 = cmul(e2, e2), e256 = cexp_(ar, ai, 256.f);
    X[1] = cmul(X[0], e1); X[2] = cmul(X[0], e2); X[3] = cmul(X[1], e2);
    X[4] = cmul(X[0], e4); X[5] = cmul(X[1], e4); X[6] = cmul(X[2], e4); X[7] = cmul(X[3], e4);
    X[8] = cmul(X[0], e256); X[9] = cmul(X[2], e256); X[10] = cmul(X[4], e256); X[11] = cmul(X[6], e256);
    X[12] = cmul(X[8], e256); X[13] = cmul(X[10], e256); X[14] = cmul(X[12], e256); X[15] = cmul(X[13], e256);
    X[16] = cmul(X[14], e256); X[17] = cmul(X[16], e256); X[18] = cmul(X[17], e256); X[19] = cmul(X[18], e256); X[20] = cmul(X[19], e256);
#pragma unroll
    for (int r = 0; r < 21; r++) putx(r, X[r]);
    cf Y[32];
    Y[0] = cf{1.f, 0.f}; Y[1] = cexp_(ar, ai, 8.f);
#pragma unroll
    for (int hb = 1; hb < 32; hb <<= 1) {
      if (hb > 1) Y[hb] = cmul(Y[hb >> 1], Y[hb >> 1]);
#pragma unroll
      for (int c = 1; c < hb; c++) Y[hb + c] = cmul(Y[c], Y[hb]);
    }
#pragma unroll
    for (int c = 0; c < 32; c++) puty(c, Y[c]);
    f32x16 acc = {};
    const int ro = (lane & 31) * KG_ROW + (lane >> 5) * 16;
#pragma unroll
    for (int s = 0; s < 8; s++) {
      bf16x8 a = *(const bf16x8*)(tx + ro + s * 32), bb = *(const bf16x8*)(ty + ro + s * 32);
      acc = __builtin_amdgcn_mfma_f32_32x32x16_bf16(a, bb, acc, 0, 0, 0);
    }
    float* part = (float*)(sm + 4 * KG_WAVE + dd * 4096);
    u16* kd = (u16*)(sm + 4 * KG_WAVE + 2 * 4096 + dd * 1408);
    if (w & 1) {
#pragma unroll
      for (int r = 0; r < 12; r++) part[r * 64 + lane] = acc[r];
    }
    __syncthreads();
    if (!(w & 1)) {
      const int c = lane & 31, h = lane >> 5;
#pragma unroll
      for (int r = 0; r < 12; r++) {
        float v = acc[r] + part[r * 64 + lane];
        int row = (r & 3) + 8 * (r >> 2) + 4 * h, idx;
        if (row < 8) idx = row + 8 * c;
        else if (row < 12) idx = 256 + (row - 8) + 4 * c;
        else if (row < 16) idx = 384 + (row & 1) + 2 * c + 64 * ((row >> 1) & 1);
        else if (row < 20) idx = 512 + 32 * (row - 16) + c;
        else idx = (row == 20 && c == 0) ? 640 : 641;
        kd[idx] = (u16)pk2(v, v);
      }
    }
    __syncthreads();
    u16* kr = (u16*)(ws + OFF_KR) + (size_t)d * 4096;
#pragma unroll
    for (int i = 0; i < 2; i++) {
      const int ch = ((w & 1) * 2 + i) * 64 + lane;
      u32 v[9];
#pragma unroll
      for (int j = 0; j < 9; j++) {
        int l = 2047 - 8 * ch - j;
        int lc = l < 0 ? 0 : l;
        int idx = lc < 128 ? lc : (int)(pk2((float)lc, 0.f) & 0xffff) - 134 * 128 + 128;
        v[j] = l < 0 ? 0u : (u32)kd[idx];
      }
      u32x4 rb = {v[0] | (v[1] << 16), v[2] | (v[3] << 16), v[4] | (v[5] << 16), v[6] | (v[7] << 16)};
      u32x4 ra = {v[1] | (v[2] << 16), v[3] | (v[4] << 16), v[5] | (v[6] << 16), v[7] | (v[8] << 16)};
      *(u32x4*)(kr + ch * 8) = ra;
      *(u32x4*)(kr + 2048 + ch * 8) = rb;
    }
  } else if (b < 320) {
    const int t0 = ((b - 256) * 4 + w) * 8;
    u32x4 v[8];
#pragma unroll
    for (int i = 0; i < 8; i++) v[i] = *(const u32x4*)(x + (size_t)(t0 + i) * D + lane * 8);
    float f[8][8], s[8];
#pragma unroll
    for (int i = 0; i < 8; i++) {
      s[i] = 0.f;
#pragma unroll
      for (int j = 0; j < 4; j++) { f[i][2 * j] = bflo(v[i][j]); f[i][2 * j + 1] = bfhi(v[i][j]); s[i] += f[i][2 * j] + f[i][2 * j + 1]; }
    }
#pragma unroll
    for (int o = 1; o < 64; o <<= 1)
#pragma unroll
      for (int i = 0; i < 8; i++) s[i] += __shfl_xor(s[i], o);
    float q[8];
#pragma unroll
    for (int i = 0; i < 8; i++) {
      s[i] *= (1.f / D); q[i] = 0.f;
#pragma unroll
      for (int j = 0; j < 8; j++) { float e = f[i][j] - s[i]; q[i] += e * e; }
    }
#pragma unroll
    for (int o = 1; o < 64; o <<= 1)
#pragma unroll
      for (int i = 0; i < 8; i++) q[i] += __shfl_xor(q[i], o);
    u32x4 o[8];
#pragma unroll
    for (int i = 0; i < 8; i++) {
      float mb = rbf(s[i]), sd = rbf(__builtin_sqrtf(rbf(rbf(q[i] * (1.f / D)) + 1e-5f))), rs = 1.f / sd;
#pragma unroll
      for (int j = 0; j < 4; j++) {
        u32 c = pk2(f[i][2 * j] - mb, f[i][2 * j + 1] - mb);
        o[i][j] = pk2(bflo(c) * rs, bfhi(c) * rs);
      }
      int t = t0 + i;
      *(u32x4*)(ws + OFF_XF + ((size_t)((t >> 5) * 32 + (lane >> 1)) * 64 + (lane & 1) * 32 + (t & 31)) * 16) = o[i];
    }
#pragma unroll
    for (int j = 0; j < 8; j++) {
      u32x4 tr;
#pragma unroll
      for (int e = 0; e < 4; e++) tr[e] = __builtin_amdgcn_perm(o[2 * e + 1][j >> 1], o[2 * e][j >> 1], (j & 1) ? 0x07060302u : 0x05040100u);
      *(u32x4*)(ws + OFF_XT + ((size_t)(lane * 8 + j) * L + t0) * 2) = tr;
    }
  } else {
    int q = (b - 320) * 256 + tid;
    if (q < 33024) {
      u32x4 v[8]; char* dst;
      if (q < 22016) {
        int mat = q >= 11008, r = q - mat * 11008, kg = r / 172, ch = r - kg * 172;
        const u16* src = (mat ? wu : wg) + (size_t)kg * 8 * F + ch * 8;
#pragma unroll
        for (int i = 0; i < 8; i++) v[i] = *(const u32x4*)(src + i * F);
        dst = ws + OFF_W1 + ((size_t)(((ch >> 2) * 2 + mat) * 32 + (kg >> 1)) * 64 + (kg & 1) * 32 + (ch & 3) * 8) * 16;
      } else {
        int r = q - 22016, kg = r >> 6, ch = r & 63;
        const u16* src = wd + (size_t)((kg >> 1) * 16 + (kg & 1) * 4) * D + ch * 8;
#pragma unroll
        for (int i = 0; i < 8; i++) v[i] = *(const u32x4*)(src + (8 * (i >> 2) + (i & 3)) * D);
        dst = ws + OFF_W2 + ((size_t)((ch >> 2) * KS2P + (kg >> 1)) * 64 + (kg & 1) * 32 + (ch & 3) * 8) * 16;
      }
#pragma unroll
      for (int j = 0; j < 8; j++) {
        u32x4 tr;
#pragma unroll
        for (int e = 0; e < 4; e++) tr[e] = __builtin_amdgcn_perm(v[2 * e + 1][j >> 1], v[2 * e][j >> 1], (j & 1) ? 0x07060302u : 0x05040100u);
        *(u32x4*)(dst + j * 16) = tr;
      }
    } else if (q < 33024 + 2048) {
      int r = q - 33024;
      u32x4 z = {0, 0, 0, 0};
      *(u32x4*)(ws + OFF_W2 + ((size_t)((r >> 7) * KS2P + KS2) * 64 + (r & 127)) * 16) = z;
    }
  }
}

constexpr int G1_STAGE = 40960;
__global__ __launch_bounds__(256, 1) void k_gemm1(char* __restrict__ ws) {
  __shared__ __attribute__((aligned(16))) char sm[4 * G1_STAGE];
  const int b = blockIdx.x, tid = threadIdx.x, lane = tid & 63, w = __builtin_amdgcn_readfirstlane(tid >> 6);
  const int xcd = b & 7, j = b >> 3;
  if (j >= 30) return;
  const int panel = 2 * xcd + (j >= 15), grp = j >= 15 ? j - 15 : j;
  const int tt = panel * 4 + w;
  const char* xsrc = ws + OFF_XF + (size_t)tt * 32768 + lane * 16;
  const char* wsrc[6];
#pragma unroll
  for (int i = 0; i < 6; i++) {
    int p = w * 6 + i, fm = p >> 2, ft = grp * 3 + (fm >> 1); ft = ft > NFT - 1 ? NFT - 1 : ft;
    wsrc[i] = ws + OFF_W1 + ((size_t)(ft * 2 + (fm & 1)) * 32 + (p & 3)) * 1024 + lane * 16;
  }
  auto issue = [&](int st) {
    char* base = sm + (st & 3) * G1_STAGE;
#pragma unroll
    for (int i = 0; i < 6; i++) glds16(wsrc[i] + st * 4096, base + 16384 + (w * 6 + i) * 1024);
#pragma unroll
    for (int i = 0; i < 4; i++) glds16(xsrc + st * 4096 + i * 1024, base + (w * 4 + i) * 1024);
  };
  f32x16 acc[6] = {};
  issue(0); issue(1); issue(2); issue(3);
#pragma unroll
  for (int st = 0; st < 8; st++) {
    if (st == 0) WAITV(30); else if (st <= 5) WAITV(20); else if (st == 6) WAITV(10); else WAITV(0);
    WAITL0(); BAR();
    if (st >= 1 && st + 3 < 8) issue(st + 3);
    const char* base = sm + (st & 3) * G1_STAGE + lane * 16;
#pragma unroll
    for (int s = 0; s < 4; s++) {
      bf16x8 xb = *(const bf16x8*)(base + (w * 4 + s) * 1024);
#pragma unroll
      for (int fm = 0; fm < 6; fm++) {
        bf16x8 wa = *(const bf16x8*)(base + 16384 + (fm * 4 + s) * 1024);
        acc[fm] = __builtin_amdgcn_mfma_f32_32x32x16_bf16(wa, xb, acc[fm], 0, 0, 0);
      }
    }
  }
#pragma unroll
  for (int f3 = 0; f3 < 3; f3++) {
    int ft = grp * 3 + f3;
    if (ft < NFT) {
      float h[16];
#pragma unroll
      for (int r = 0; r < 16; r++) {
        float g = acc[2 * f3][r], u = acc[2 * f3 + 1][r];
        float e = __builtin_amdgcn_exp2f(g * -1.44269504088896341f);
        h[r] = g * u * __builtin_amdgcn_rcpf(1.f + e);
      }
#pragma unroll
      for (int s = 0; s < 2; s++) {
        u32x4 o = {pk2(h[8 * s], h[8 * s + 1]), pk2(h[8 * s + 2], h[8 * s + 3]), pk2(h[8 * s + 4], h[8 * s + 5]), pk2(h[8 * s + 6], h[8 * s + 7])};
        *(u32x4*)(ws + OFF_HF + ((size_t)(tt * KS2 + ft * 2 + s) * 64 + lane) * 16) = o;
      }
    }
  }
}

constexpr int CV_UA = 0, CV_UB = 4640, CV_RA = 9344, CV_RB = 13568, CV_D = 17920;
constexpr int K2_RED = 2 * CV_D;
constexpr int K2_Y = K2_RED + 65536;
constexpr int K2_CNT = K2_Y + 8192;
constexpr int K2_LDS = K2_CNT + 16;
constexpr int PF = 8;

template <int HALF>
DEVI void conv_main(const char* ub, const char* tb, f32x4 (&acc)[4]) {
  constexpr int TI[4] = {HALF ? 6 : 7, HALF ? 5 : 4, HALF ? 2 : 3, HALF ? 1 : 0};
  constexpr int NF = HALF ? 7 : 8;
#pragma unroll 1
  for (int r = 0; r < 8; r++) {
    bf16x8 U[NF], T[NF];
#pragma unroll
    for (int n = 0; n < NF; n++) {
      U[n] = *(const bf16x8*)(ub - r * 64 + n * 512);
      bf16x4 lo = *(const bf16x4*)(tb - r * 64 - n * 512), hi = *(const bf16x4*)(tb - r * 64 - n * 512 + 8);
      T[n] = __builtin_shufflevector(lo, hi, 0, 1, 2, 3, 4, 5, 6, 7);
    }
#pragma unroll
    for (int n = 0; n < NF; n++)
#pragma unroll
      for (int a = 0; a < 4; a++)
        if (TI[a] - n >= 0) acc[a] = __builtin_amdgcn_mfma_f32_16x16x32_bf16(T[TI[a] - n], U[n], acc[a], 0, 0, 0);
  }
}

__global__ __launch_bounds__(512, 2) void k_gemm2(const u16* __restrict__ x, const u16* __restrict__ dsk, char* __restrict__ ws, u16* __restrict__ out) {
  __shared__ __attribute__((aligned(16))) char sm[K2_LDS];
  const int b = blockIdx.x, tid = threadIdx.x, lane = tid & 63, w = __builtin_amdgcn_readfirstlane(tid >> 6);
  const int xcd = b & 7, j = b >> 3, tb = 4 * xcd + (j >> 3), dg = j & 7;
  if (w < 4) {
    const char* hsrc = ws + OFF_HF + (size_t)(tb * 2) * KS2 * 1024 + lane * 16;
    const char* wsrc = ws + OFF_W2 + ((size_t)(dg * 2) * KS2P + w * 22) * 1024 + lane * 16;
    bf16x8 A[PF][2], B[PF][2];
    auto load = [&](int i, int sl) {
      int s = w * 22 + i; s = s > KS2 - 1 ? KS2 - 1 : s;
      A[sl][0] = *(const bf16x8*)(wsrc + i * 1024);
      B[sl][0] = *(const bf16x8*)(hsrc + s * 1024);
      A[sl][1] = *(const bf16x8*)(wsrc + (KS2P + i) * 1024);
      B[sl][1] = *(const bf16x8*)(hsrc + (KS2 + s) * 1024);
    };
#pragma unroll
    for (int i = 0; i < PF; i++) load(i, i);
    const int dt = w & 1, ttl = w >> 1;
    const int trow = tb * 64 + ttl * 32 + (lane & 31), dcol = dg * 64 + dt * 32 + (lane >> 5) * 4;
    u32x2 xr[4];
#pragma unroll
    for (int g = 0; g < 4; g++) xr[g] = *(const u32x2*)(x + (size_t)trow * D + dcol + g * 8);
    BAR();
    f32x16 acc[4] = {};
#pragma unroll
    for (int i = 0; i < 22; i++) {
      const int sl = i % PF;
      acc[0] = __builtin_amdgcn_mfma_f32_32x32x16_bf16(A[sl][0], B[sl][0], acc[0], 0, 0, 0);
      acc[1] = __builtin_amdgcn_mfma_f32_32x32x16_bf16(A[sl][1], B[sl][0], acc[1], 0, 0, 0);
      acc[2] = __builtin_amdgcn_mfma_f32_32x32x16_bf16(A[sl][0], B[sl][1], acc[2], 0, 0, 0);
      acc[3] = __builtin_amdgcn_mfma_f32_32x32x16_bf16(A[sl][1], B[sl][1], acc[3], 0, 0, 0);
      if (i + PF < 22) load(i + PF, sl);
      __builtin_amdgcn_sched_barrier(0);
    }
#pragma unroll
    for (int tl = 0; tl < 4; tl++)
#pragma unroll
      for (int g = 0; g < 4; g++) {
        f32x4 v = {acc[tl][4 * g], acc[tl][4 * g + 1], acc[tl][4 * g + 2], acc[tl][4 * g + 3]};
        *(f32x4*)(sm + K2_RED + ((tl * 4 + w) * 4 + g) * 1024 + lane * 16) = v;
      }
    WAITL0(); BAR();
    const u32 ybase = (u32)(size_t)(sm + K2_Y + w * 2048) + ((lane >> 5) * 4 + ((lane & 15) >> 2)) * 64 + ((lane >> 4) & 1) * 32 + (lane & 3) * 8;
#pragma unroll
    for (int g = 0; g < 4; g++) {
      f32x4 v = *(const f32x4*)(sm + K2_RED + ((w * 4 + 0) * 4 + g) * 1024 + lane * 16);
#pragma unroll
      for (int ww = 1; ww < 4; ww++) v += *(const f32x4*)(sm + K2_RED + ((w * 4 + ww) * 4 + g) * 1024 + lane * 16);
      u32x2 y;
      asm volatile("ds_read_b64_tr_b16 %0, %1\n\ts_waitcnt lgkmcnt(0)" : "=&v"(y) : "v"(ybase + g * 512) : "memory");
      u32 f0 = pk2(v[0], v[1]), f1 = pk2(v[2], v[3]);
      u32 r0 = pk2(bflo(xr[g][0]) + bflo(y[0]), bfhi(xr[g][0]) + bfhi(y[0]));
      u32 r1 = pk2(bflo(xr[g][1]) + bflo(y[1]), bfhi(xr[g][1]) + bfhi(y[1]));
      u32x2 o = {pk2(bflo(r0) + bflo(f0), bfhi(r0) + bfhi(f0)), pk2(bflo(r1) + bflo(f1), bfhi(r1) + bfhi(f1))};
      *(u32x2*)(out + (size_t)trow * D + dcol + g * 8) = o;
    }
  } else {
    const int cw = w - 4, dd = cw >> 1, half = cw & 1, d = 2 * b + dd;
    char* cb = sm + dd * CV_D;
    if (half == 0) {
      const char* src = ws + OFF_XT + (size_t)d * 4096 + lane * 16;
#pragma unroll
      for (int i = 0; i < 4; i++) { glds16(src + i * 1024, cb + CV_UA + 512 + i * 1024); glds16(src + i * 1024, cb + CV_UB + 516 + i * 1024); }
      u32x4 z = {0, 0, 0, 0};
      *(u32x4*)(cb + (lane < 32 ? CV_UA : CV_UB - 512) + lane * 16) = z;
      if (lane == 0) *(u32*)(cb + CV_UB + 512) = 0u;
    } else {
      const char* src = ws + OFF_KR + (size_t)d * 8192 + lane * 16;
#pragma unroll
      for (int i = 0; i < 4; i++) { glds16(src + i * 1024, cb + CV_RA + i * 1024); glds16(src + 4096 + i * 1024, cb + CV_RB + i * 1024); }
      if (lane < 32) *(u32*)(cb + (lane < 16 ? CV_RA : CV_RB - 64) + 4096 + lane * 4) = 0u;
      if (cw == 1 && lane == 0) *(u32*)(sm + K2_CNT) = 0u;
    }
    const float dskip = bf1(dsk[d]);
    WAITV(0); WAITL0(); BAR();
    const int c = lane & 15, q = lane >> 4;
    const char* ub = cb + ((c & 1) ? CV_UA : CV_UB) + 2 * (256 + 32 * (c >> 1) + 8 * q);
    const char* tbp = cb + ((c & 1) ? CV_RB : CV_RA) + 2 * (2044 - 4 * (c >> 1) + 8 * q);
    f32x4 acc[4] = {};
    if (half) conv_main<1>(ub, tbp, acc); else conv_main<0>(ub, tbp, acc);
    u32 p[4][2];
    const int toff = 8 * q + 2 * (c & 1) + 32 * (c >> 1);
#pragma unroll
    for (int a = 0; a < 4; a++) {
      int ti = (a == 0 ? 7 : a == 1 ? 4 : a == 2 ? 3 : 0) ^ half;
#pragma unroll
      for (int e = 0; e < 2; e++) {
        u32 u2 = *(const u32*)(cb + CV_UA + 2 * (256 + 256 * ti + toff + 4 * e));
        u32 yb = pk2(acc[a][2 * e], acc[a][2 * e + 1]);
        u32 sk = pk2(bflo(u2) * dskip, bfhi(u2) * dskip);
        p[a][e] = pk2(bflo(yb) + bflo(sk), bfhi(yb) + bfhi(sk));
      }
    }
    auto rsrc = __builtin_amdgcn_make_buffer_rsrc(ws + OFF_YT, 0, 2u << 20, 0x00020000);
#pragma unroll
    for (int a = 0; a < 4; a += 2) {
      const bool odd = c & 1;
      u32 s0 = odd ? p[a][0] : p[a + 1][0], s1 = odd ? p[a][1] : p[a + 1][1];
      u32 r0 = __builtin_amdgcn_mov_dpp(s0, 0xB1, 0xf, 0xf, true), r1 = __builtin_amdgcn_mov_dpp(s1, 0xB1, 0xf, 0xf, true);
      u32x4 o;
      if (odd) o = u32x4{r0, p[a + 1][0], r1, p[a + 1][1]}; else o = u32x4{p[a][0], r0, p[a][1], r1};
      int tiA = (a == 0 ? 7 : 3) ^ half, tiB = (a == 0 ? 4 : 0) ^ half;
      int t = 256 * (odd ? tiB : tiA) + 8 * q + 32 * (c >> 1);
      __builtin_amdgcn_raw_buffer_store_b128(o, rsrc, (d * L + t) * 2, 0, 16);
    }
    WAITV(0);
    u32 old = 0;
    if (lane == 0) old = __hip_atomic_fetch_add((LAS u32*)(sm + K2_CNT), 1u, __ATOMIC_RELAXED, __HIP_MEMORY_SCOPE_WORKGROUP);
    old = __builtin_amdgcn_readfirstlane(old);
    GAS u32* flags = (GAS u32*)(ws + OFF_FL);
    if (old == 3 && lane == 0) __hip_atomic_store(flags + b * 32, 1u, __ATOMIC_RELAXED, __HIP_MEMORY_SCOPE_AGENT);
    const int d0 = dg * 64 + (cw & 1) * 32, t0 = tb * 64 + (cw >> 1) * 32;
    for (int spin = 0; spin < (1 << 20); spin++) {
      u32 fl = __hip_atomic_load(flags + ((d0 >> 1) + (lane & 15)) * 32, __ATOMIC_RELAXED, __HIP_MEMORY_SCOPE_AGENT);
      if (__all(fl == 1u)) break;
      __builtin_amdgcn_s_sleep(2);
    }
    __builtin_amdgcn_fence(__ATOMIC_ACQUIRE, "wavefront");
#pragma unroll
    for (int i = 0; i < 2; i++) {
      int row = i * 16 + (lane >> 2);
      u32x4 v = __builtin_amdgcn_raw_buffer_load_b128(rsrc, ((d0 + row) * L + t0 + (lane & 3) * 8) * 2, 0, 16);
      *(u32x4*)(sm + K2_Y + cw * 2048 + row * 64 + (lane & 3) * 16) = v;
    }
    WAITL0(); BAR();
  }
}

extern "C" void kernel_launch(void* const* d_in, const int* in_sizes, int n_in, void* d_out, int out_size, void* d_ws, size_t ws_size, hipStream_t stream) {
  const u16* const* in = (const u16* const*)d_in;
  char* ws = (char*)d_ws;
  k_prep<<<320 + 137, 256, 0, stream>>>(in[0], in[1], in[2], in[3], in[4], in[5], in[6], in[7], in[10], in[11], in[12], ws);
  k_gemm1<<<256, 256, 0, stream>>>(ws);
  k_gemm2<<<256, 512, 0, stream>>>(in[0], in[8], ws, (u16*)d_out);
}
```

```cpp
#include <hip/hip_runtime.h>
#include <hip/hip_bf16.h>
#include <stdint.h>

typedef unsigned short u16;
typedef unsigned int u32;
typedef __attribute__((ext_vector_type(8))) short bf16x8;
typedef __attribute__((ext_vector_type(4))) short s16x4;
typedef __attribute__((ext_vector_type(4))) float f32x4;
typedef __attribute__((ext_vector_type(16))) float f32x16;
typedef __attribute__((ext_vector_type(4))) u32 u32x4;
typedef __attribute__((ext_vector_type(2))) u32 u32x2;
typedef __attribute__((ext_vector_type(2))) float f32x2;
typedef __attribute__((ext_vector_type(2))) __bf16 bf2_t;
#define LAS __attribute__((address_space(3)))
#define DEVI __device__ __forceinline__

constexpr int L = 2048, D = 512, NS = 128, F = 1376;
constexpr int KS2 = 86;
constexpr u32 OFF_XT = 0;
constexpr u32 OFF_YT = 4u << 20;
constexpr u32 OFF_HF = 6u << 20;
constexpr u32 OFF_ST = 12u << 20;
constexpr u32 OFF_CS = OFF_ST + 65536;
constexpr u32 OFF_EP = OFF_CS + 32 * 8 * 192 * 8;
constexpr u32 OFF_F2 = OFF_EP + 4096;
constexpr u32 WS_USED = 13u << 20;

DEVI float bflo(u32 v) { return __builtin_bit_cast(float, v << 16); }
DEVI float bfhi(u32 v) { return __builtin_bit_cast(float, v & 0xffff0000u); }
DEVI u32 pk2(float lo, float hi) { f32x2 f = {lo, hi}; bf2_t b = __builtin_convertvector(f, bf2_t); return __builtin_bit_cast(u32, b); }
DEVI float rbf(float f) { return bflo(pk2(f, f)); }
DEVI bool wall(bool ok) { return __builtin_amdgcn_ballot_w64(!ok) == 0ull; }
#define WAITV(n) asm volatile("s_waitcnt vmcnt(" #n ")" ::: "memory")
#define FBAR() asm volatile("s_waitcnt lgkmcnt(0)\n\ts_barrier" ::: "memory")
#define VBAR(n) asm volatile("s_waitcnt vmcnt(" #n ") lgkmcnt(0)\n\ts_barrier" ::: "memory")
#define SCHED() __builtin_amdgcn_sched_barrier(0)
#define PIN(v) asm volatile("" : "+v"(v))
#define MFMA32(a, b, c) __builtin_amdgcn_mfma_f32_32x32x16_bf16(a, b, c, 0, 0, 0)
#define MFMA16(a, b, c) __builtin_amdgcn_mfma_f32_16x16x32_bf16(a, b, c, 0, 0, 0)
#define PERM(hi, lo) __builtin_amdgcn_perm(hi, lo, 0x05040100u)
#define SC1 16
#define OOB 0x80000000u
#define ROT dg
#define GST(s)
#define PST(i) STAMP(i)
template <int N> struct IC { static constexpr int value = N; };

#define DIAG_DECL
#define STAMP(i)

typedef f32x2 cf;
DEVI cf cmul(cf b, cf a) { const cf t = cf{a[0], a[0]} * b; return __builtin_elementwise_fma(cf{-a[1], a[1]}, cf{b[1], b[0]}, t); }
DEVI u32 pkc(cf v) { return __builtin_bit_cast(u32, __builtin_convertvector(v, bf2_t)); }
DEVI cf cexp2_(float lr, float rev, float x) {
  float e = __builtin_amdgcn_exp2f(lr * x), f = __builtin_amdgcn_fractf(rev * x);
  return cf{e, e} * cf{__builtin_amdgcn_cosf(f), __builtin_amdgcn_sinf(f)};
}
DEVI u32x4 mkrsrc(const void* p, u32 bytes) { u32x4 r = {(u32)(size_t)p, (u32)((size_t)p >> 32) & 0xffffu, bytes, 0x00020000u}; return r; }
template <int IMM>
DEVI void dma16(u32 lds, u32 voff, u32x4 rsrc, u32 soff) {
  asm volatile("s_add_u32 m0, %0, %4\n\ts_nop 0\n\tbuffer_load_dwordx4 %1, %2, %3 offen lds" :: "s"(lds), "v"(voff), "s"(rsrc), "s"(soff), "i"(IMM) : "scc");
}
DEVI u32x2 trread(const LAS char* p) { return __builtin_bit_cast(u32x2, __builtin_amdgcn_ds_read_tr16_b64_v4i16((LAS s16x4*)p)); }
template <int STEP>
DEVI bf16x8 trfrag(const LAS char* p) {
  s16x4 lo = __builtin_amdgcn_ds_read_tr16_b64_v4i16((LAS s16x4*)p), hi = __builtin_amdgcn_ds_read_tr16_b64_v4i16((LAS s16x4*)(p + STEP));
  return __builtin_shufflevector(lo, hi, 0, 1, 2, 3, 4, 5, 6, 7);
}
template <int CTRL, int RM> DEVI float dppadd(float v) { return v + __builtin_bit_cast(float, __builtin_amdgcn_update_dpp(0, __builtin_bit_cast(int, v), CTRL, RM, 0xf, true)); }
DEVI float lane63(float v) { return __builtin_bit_cast(float, __builtin_amdgcn_readlane(__builtin_bit_cast(int, v), 63)); }
DEVI float dot2(u32 a, u32 b, float c) { return __builtin_amdgcn_fdot2_f32_bf16(__builtin_bit_cast(bf2_t, a), __builtin_bit_cast(bf2_t, b), c, false); }

constexpr int PART = 10368, P_G = 4096, P_U = 7200;
constexpr int STG = 4 * PART;
constexpr int CV0 = 3 * STG;
constexpr int CV_UA = 0, CV_UB = 4640, CV_RA = 9344, CV_RB = 13568, CV_D = 17920;
constexpr int KX = 5888, KY = 8704, CV_LN = 2 * KX;
constexpr int MISC = CV0 + 2 * CV_D;
constexpr int M_P = MISC;
constexpr int M_KD = MISC + 48;
constexpr int KDR = 20, KDS = 32 * KDR * 2 + 16;
constexpr int M_CS = M_KD + 2 * KDS;
constexpr int LDS_BYTES = M_CS + 768;
constexpr int SPIN_L = 1 << 18, SPIN_G = 1 << 16;

DEVI void lds_post(LAS char* sm, int word_off, u32 v) { if ((threadIdx.x & 63) == 0) *(volatile LAS u32*)(sm + word_off) = v; }
template <int N>
DEVI void lds_wait(LAS char* sm, int off, u32 v) {
#pragma unroll 1
  for (int i = 0; i < SPIN_L; i++) {
    u32 m;
    if (N == 1) m = *(volatile LAS u32*)(sm + off);
    else if (N == 2) { u32x2 p = *(volatile LAS u32x2*)(sm + off); m = p[0] < p[1] ? p[0] : p[1]; }
    else { u32x4 p = *(volatile LAS u32x4*)(sm + off); u32 a = p[0] < p[1] ? p[0] : p[1], b = p[2] < p[3] ? p[2] : p[3]; m = a < b ? a : b; }
    if ((u32)__builtin_amdgcn_readfirstlane(m) >= v) break;
    __builtin_amdgcn_s_sleep(1);
  }
  asm volatile("" ::: "memory");
}
#define ALD16(dst, base, voff) asm volatile("global_load_ushort %0, %1, %2" : "=v"(dst) : "v"(voff), "s"(base))
#define ALD128(dst, base, voff) asm volatile("global_load_dwordx4 %0, %1, %2" : "=v"(dst) : "v"(voff), "s"(base))
DEVI void aldw64(u32x2& dst, u32 voff, const char* base) { asm volatile("global_load_dwordx2 %0, %1, %2 sc1" : "+v"(dst) : "v"(voff), "s"(base)); }
template <int IMM> DEVI void aldw128(u32x4& dst, u32 voff, const char* base) { asm volatile("global_load_dwordx4 %0, %1, %2 offset:%3 sc1" : "+v"(dst) : "v"(voff), "s"(base), "i"(IMM)); }
template <int IMM> DEVI void aldw64o(u32x2& dst, u32 voff, const char* base) { asm volatile("global_load_dwordx2 %0, %1, %2 offset:%3 sc1" : "+v"(dst) : "v"(voff), "s"(base), "i"(IMM)); }
DEVI void wait0(u32x2& a, u32x2 (&c)[4]) { asm volatile("s_waitcnt vmcnt(0)" : "+v"(a), "+v"(c[0]), "+v"(c[1]), "+v"(c[2]), "+v"(c[3]) :: "memory"); }
DEVI void bar5(u32x2& a, u32x2 (&c)[4]) { asm volatile("s_waitcnt vmcnt(5) lgkmcnt(0)\n\ts_barrier" : "+v"(a), "+v"(c[0]), "+v"(c[1]), "+v"(c[2]), "+v"(c[3]) :: "memory"); }
DEVI void aldpark(u32x4& dst, u32 voff, u32x4 rsrc, u32 soff) { asm volatile("buffer_load_dwordx4 %0, %1, %2, %3 offen" : "+v"(dst) : "v"(voff), "s"(rsrc), "s"(soff)); }
#define ALDW64(dst, voff) aldw64(dst, voff, ws)
#define ALDW128(dst, voff, imm) aldw128<imm>(dst, voff, ws)

DEVI void conv_main(const LAS char* ub, const LAS char* tb, f32x4 (&acc)[8]) {
  auto rdU = [&](int r, int n) { return *(const LAS bf16x8*)(ub + (3 - r) * 64 + n * 512); };
  auto rdT = [&](int r, int n) {
    s16x4 lo = *(const volatile LAS s16x4*)(tb + (3 - r) * 64 + (7 - n) * 512), hi = *(const volatile LAS s16x4*)(tb + (3 - r) * 64 + (7 - n) * 512 + 8);
    return (bf16x8)__builtin_shufflevector(lo, hi, 0, 1, 2, 3, 4, 5, 6, 7);
  };
  bf16x8 U[2][8], T[2][8];
  U[0][0] = rdU(0, 0);
#pragma unroll
  for (int n = 0; n < 8; n++) T[0][n] = rdT(0, n);
#pragma unroll
  for (int n = 1; n < 8; n++) U[0][n] = rdU(0, n);
#pragma unroll
  for (int r = 0; r < 4; r++) {
    const int p = r & 1;
    int k = 0;
#pragma unroll
    for (int n = 0; n < 8; n++)
#pragma unroll
      for (int t = 0; t + n < 8; t++) {
        acc[t + n] = MFMA16(T[p][t], U[p][n], acc[t + n]);
        if (r < 3 && k < 32 && (k & 1)) { if (k < 16) T[p ^ 1][k >> 1] = rdT(r + 1, k >> 1); else U[p ^ 1][(k >> 1) - 8] = rdU(r + 1, (k >> 1) - 8); }
        k++;
        SCHED();
      }
  }
}

__global__ __launch_bounds__(512, 2) void k_fused(const u16* __restrict__ x, const u16* __restrict__ Are, const u16* __restrict__ Aim,
    const u16* __restrict__ Bre, const u16* __restrict__ Bim, const u16* __restrict__ Cre, const u16* __restrict__ Cim,
    const u16* __restrict__ ldt, const u16* __restrict__ dsk, const u16* __restrict__ wg, const u16* __restrict__ wu,
    const u16* __restrict__ wd, char* ws, u16* __restrict__ out) {
  __shared__ __attribute__((aligned(16))) char smem[LDS_BYTES];
  LAS char* sm = (LAS char*)smem;
  const int b = blockIdx.x, tid = threadIdx.x, lane = tid & 63, w = __builtin_amdgcn_readfirstlane(tid >> 6);
  const int xcd = b & 7, j = b >> 3;
  DIAG_DECL STAMP(0);
  const int panel = (xcd >> 2) * 8 + (j & 7), grp = (xcd & 3) * 4 + (j >> 3);
  const int tb = 4 * xcd + (j >> 3), dg = j & 7;
  const int idx = 32 * xcd + j;
  const int tq = w & 3, fh = w >> 2;
  const int r32 = lane & 31, h = lane >> 5, c16 = lane & 15, q = lane >> 4, q4 = c16 >> 2;
  asm volatile("" :: "s"(wg), "s"(wu), "s"(wd), "s"(x), "s"(ws), "s"(dsk), "s"(out));
  u32 ep;
  asm volatile("s_load_dword %0, %1, 0x0" : "=s"(ep) : "s"(ws + OFF_EP));
  u32x4 xv; u32 p_ar, p_ai, p_br, p_bi, p_cr, p_ci, p_dt, p_ds;
  {
    const int dt_ = 2 * idx + (tq >> 1);
    const u32 g2 = (u32)(dt_ * NS + (w & 1) * 64 + lane) * 2u, d2 = (u32)dt_ * 2u;
    ALD16(p_ar, Are, g2); ALD16(p_ai, Aim, g2); ALD16(p_dt, ldt, d2);
    ALD128(xv, x, (u32)((idx * 8 + w) * 1024 + lane * 16));
    ALD16(p_br, Bre, g2); ALD16(p_bi, Bim, g2); ALD16(p_cr, Cre, g2); ALD16(p_ci, Cim, g2); ALD16(p_ds, dsk, d2);
  }
  SCHED(); __builtin_amdgcn_s_barrier(); SCHED();
  auto rws = __builtin_amdgcn_make_buffer_rsrc(ws, 0, WS_USED, 0x00020000);
  __builtin_amdgcn_raw_buffer_store_b128(u32x4{~0u, ~0u, ~0u, ~0u}, rws, (u32)(OFF_YT + idx * 8192 + tid * 16), 0, SC1);
  SCHED();
  const u32x4 rw = mkrsrc(fh ? wu : wg, grp < 15 ? D * F * 2 : 0), rw2 = mkrsrc(fh ? wu : wg, grp < 14 ? D * F * 2 : 0);
  const u32 wo = (u32)((tq * 16 + (lane >> 2)) * (F * 2) + grp * 192 + (lane & 3) * 16);
  const u32 lx = (u32)(size_t)sm + tq * PART + fh * 2048, lw = (u32)(size_t)sm + tq * PART + (fh ? P_U : P_G);
  auto sow = [&](int st) { return (u32)(((st + ROT) & 7) * (64 * F * 2)); };
  dma16<0>(lw, wo, rw, sow(0)); dma16<1024>(lw, wo, rw2, sow(0) + 64); dma16<2048>(lw, wo, rw2, sow(0) + 128);
  SCHED();
  const u32x4 rx = mkrsrc(x, grp < 15 ? L * D * 2 : 0);
  u32 xo = (u32)(((panel * 4 + tq) * 32 + fh * 16 + (lane >> 3)) * 1024 + (((lane & 7) ^ q) * 16)), xo2 = (xo ^ 64u) + 8192u;
  auto sox = [&](int st) { return (u32)(((st + ROT) & 7) * 128); };
  const int th = w & 1, kh = tq >> 1;
  u32 wdo = (u32)((th * 8 + (lane >> 3)) * 1024 + dg * 128 + (((lane & 7) ^ ((q & 1) << 2)) * 16));
  PIN(xo); PIN(xo2); PIN(wdo);
  auto pdma = [&](int i) {
    SCHED();
    if (i == 0) dma16<0>(lx, xo, rx, sox(0));
    if (i == 1) dma16<1024>(lx, xo2, rx, sox(0));
    if (i == 2) dma16<STG>(lw, wo, rw, sow(1));
    if (i == 3) dma16<STG + 1024>(lw, wo, rw2, sow(1) + 64);
    if (i == 4) dma16<STG + 2048>(lw, wo, rw2, sow(1) + 128);
    if (i == 5) dma16<STG>(lx, xo, rx, sox(1));
    if (i == 6) dma16<STG + 1024>(lx, xo2, rx, sox(1));
    SCHED();
  };
  LAS char* cb = sm + CV0 + fh * CV_D;
  const LAS char* xa[2][4]; const LAS char* wa[2];
  xa[0][0] = sm + tq * PART + r32 * 128 + ((h ^ ((r32 >> 1) & 7)) * 16);
  wa[0] = sm + ((q & 1) ? P_U : P_G) + (8 * h + q4) * 64 + (lane & 3) * 8 + fh * (1024 + 32);
  const int fo = c16 * 272 + h * 16;
  const LAS char* fxa = cb + (tq >> 1) * 16 * 272 + (q & 1) * KX + fo;
  const LAS char* fya = sm + 2 * STG + fh * 2 * KY + (tq & 1) * 16 * 272 + (q & 1) * KY + fo;
  const LAS char* xta = sm + CV0 + (q & 1) * CV_D + CV_LN + q4 * 1040 + (w * 64 + h * 16 + (lane & 3) * 4) * 2;
  u32 xto = OFF_XT + (u32)((w * 16 + h * 4 + q4) * 32768 + idx * 128 + (q & 1) * 64 + (lane & 3) * 16);
  PIN(xa[0][0]); PIN(wa[0]); PIN(fxa); PIN(fya); PIN(xta); PIN(xto);
  asm volatile("s_waitcnt vmcnt(4) lgkmcnt(0)" : "+s"(ep), "+v"(xv), "+v"(p_ar), "+v"(p_ai), "+v"(p_dt), "+v"(p_br), "+v"(p_bi), "+v"(p_cr), "+v"(p_ci), "+v"(p_ds));
  ep += 1u;
  STAMP(1);
#define tag (u32x2{ep, ep ^ 0x5bd1e995u})
#define TAGOK(v) ((bool)(((v)[0] == ep) & ((v)[1] == (ep ^ 0x5bd1e995u))))
  pdma(0);
  {
    float s = 0.f, qq = 0.f;
#pragma unroll
    for (int k = 0; k < 4; k++) { const u32 v = xv[k]; s = dot2(v, 0x3f803f80u, s); qq = dot2(v, v, qq); }
    s = dppadd<0x111, 0xf>(s); qq = dppadd<0x111, 0xf>(qq); s = dppadd<0x112, 0xf>(s); qq = dppadd<0x112, 0xf>(qq);
    s = dppadd<0x114, 0xf>(s); qq = dppadd<0x114, 0xf>(qq); s = dppadd<0x118, 0xf>(s); qq = dppadd<0x118, 0xf>(qq);
    s = dppadd<0x142, 0xa>(s); qq = dppadd<0x142, 0xa>(qq); s = dppadd<0x143, 0xc>(s); qq = dppadd<0x143, 0xc>(qq);
    const float m = s * (1.f / D), var = __builtin_fmaxf(__builtin_fmaf(-m, m, qq * (1.f / D)), 0.f);
    const u32 mv = pk2(m, var);
    const float sd = rbf(__builtin_amdgcn_sqrtf(rbf(bfhi(mv) + 1e-5f))), rs_ = __builtin_amdgcn_rcpf(sd);
    const float rs = lane63(rs_), nb = lane63(-bflo(mv) * rs_);
    pdma(1);
    u32x4 o;
#pragma unroll
    for (int k = 0; k < 4; k++) { const u32 v = xv[k]; o[k] = pk2(__builtin_fmaf(bflo(v), rs, nb), __builtin_fmaf(bfhi(v), rs, nb)); }
    *(LAS u32x4*)(cb + CV_LN + tq * 1040 + lane * 16) = o;
    if (lane == 63) { __builtin_amdgcn_raw_buffer_store_b64(u32x2{PERM(__builtin_bit_cast(u32, sd) >> 16, mv), ep}, rws, 0, OFF_ST + (idx * 8 + w) * 8, SC1); *(LAS u32*)(sm + M_P + w * 4) = 0u; }
  }
  pdma(2);
  {
    const float dt = rbf(__builtin_amdgcn_exp2f(bflo(p_dt) * 1.44269504088896341f));
    const float lr = dt * bflo(p_ar) * 1.44269504088896341f, rev = dt * bflo(p_ai) * 0.15915494309189535f;
    if (fh == 0) {
      LAS char* ty = sm + 2 * STG + tq * KY + lane * 4;
      cf Y[16];
      Y[0] = cf{1.f, 0.f}; Y[1] = cexp2_(lr, rev, 8.f);
#pragma unroll
      for (int hb = 1; hb < 16; hb <<= 1) {
        if (hb > 1) Y[hb] = cmul(Y[hb >> 1], Y[hb >> 1]);
#pragma unroll
        for (int c = 1; c < hb; c++) Y[hb + c] = cmul(Y[c], Y[hb]);
      }
      const cf y16 = cmul(Y[8], Y[8]);
      pdma(3);
#pragma unroll
      for (int c = 0; c < 16; c++) {
        cf t = c ? cmul(Y[c], y16) : y16;
        *(LAS u32*)(ty + c * 272) = pkc(Y[c]);
        *(LAS u32*)(ty + (16 + c) * 272) = pkc(t);
        if (c == 5) pdma(4);
        if (c == 11) pdma(5);
      }
    } else {
      LAS char* tx = sm + CV0 + (tq >> 1) * CV_D + (w & 1) * KX + lane * 4;
      auto put = [&](int r, cf t) { *(LAS u32*)(tx + r * 272) = pkc(t); };
      const cf Bc = {dt * bflo(p_br), -dt * bflo(p_bi)}, Cc = {bflo(p_cr), -bflo(p_ci)};
      cf X[21];
      X[0] = cmul(Cc, Bc);
      cf e1 = cexp2_(lr, -rev, 1.f), e2 = cmul(e1, e1), e4 = cmul(e2, e2), e256 = cexp2_(lr, -rev, 256.f);
      pdma(3);
      X[1] = cmul(X[0], e1); X[2] = cmul(X[0], e2); X[3] = cmul(X[1], e2);
      X[4] = cmul(X[0], e4); X[5] = cmul(X[1], e4); X[6] = cmul(X[2], e4); X[7] = cmul(X[3], e4);
#pragma unroll
      for (int r = 0; r < 8; r++) put(r, X[r]);
      pdma(4);
      X[8] = cmul(X[0], e256); X[9] = cmul(X[2], e256); X[10] = cmul(X[4], e256); X[11] = cmul(X[6], e256);
      X[12] = cmul(X[8], e256); X[13] = cmul(X[10], e256); X[14] = cmul(X[12], e256); X[15] = cmul(X[13], e256);
#pragma unroll
      for (int r = 8; r < 16; r++) put(r, X[r]);
      pdma(5);
      X[16] = cmul(X[14], e256); X[17] = cmul(X[16], e256); X[18] = cmul(X[17], e256); X[19] = cmul(X[18], e256); X[20] = cmul(X[19], e256);
#pragma unroll
      for (int r = 16; r < 21; r++) put(r, X[r]);
    }
  }
  pdma(6);
  SCHED(); VBAR(6); SCHED();
  STAMP(2);
  const u32x4 rd = mkrsrc(wd, F * D * 2);
  {
    const u32x2 xt0 = trread(xta), xt1 = trread(xta + 64);
    bf16x8 fx[8], fy[8];
#pragma unroll
    for (int k = 0; k < 8; k++) { fx[k] = *(const LAS bf16x8*)(fxa + k * 32); fy[k] = *(const LAS bf16x8*)(fya + k * 32); }
    __builtin_amdgcn_raw_buffer_store_b128(u32x4{xt0[0], ep, xt0[1], ep}, rws, xto, 0, SC1);
    __builtin_amdgcn_raw_buffer_store_b128(u32x4{xt1[0], ep, xt1[1], ep}, rws, xto + 8 * 32768, 0, SC1);
    f32x4 ka = {};
#pragma unroll
    for (int k = 0; k < 8; k++) ka = MFMA16(fx[k], fy[k], ka);
    const int kc = c16 + 16 * (tq & 1), kr = (tq >> 1) * 16 + 4 * q;
    if (kr < 20 || (kr == 20 && kc == 0)) *(LAS u32x2*)(sm + M_KD + fh * KDS + (kr < 20 ? kc * (KDR * 2) + kr * 2 : 32 * KDR * 2)) = u32x2{pk2(ka[0], ka[1]), pk2(ka[2], ka[3])};
  }
#pragma unroll
  for (int k = 1; k < 4; k++) xa[0][k] = (const LAS char*)((size_t)xa[0][0] ^ (size_t)(k * 32));
  const int go1 = fh ? 1024 - 32 : 32;
  const LAS char* wb[2] = {wa[0] + go1, nullptr};
  auto rdx = [&](int st, int k) { return *(const LAS bf16x8*)(xa[st % 3 == 2][k] + (st % 3 == 1 ? STG : 0)); };
  auto rdw = [&](int st, int k, int g) {
    const int o = (st % 3) * STG + k * PART, hi = o >= 6 * PART;
    return trfrag<256>((g == 1 ? wb[hi] : wa[hi]) + (o - hi * 6 * PART) + (g == 2 ? 1024 : 0));
  };
  bf16x8 X = rdx(0, 0), W[3] = {rdw(0, 0, 0), rdw(0, 0, 1), rdw(0, 0, 2)};
  f32x16 acc[3] = {};
  f32x4 cs[3] = {};
  bf16x8 csel;
  { const u32 o = (c16 == (q & 1) * 4) ? 0x3f803f80u : 0u; csel = __builtin_bit_cast(bf16x8, u32x4{o, o, o, o}); }
  SCHED(); FBAR(); SCHED();
  STAMP(3);
  u32 o_st, o_u, o_cs;
  const u32 ld = (u32)(size_t)sm + kh * 20480 + fh * 10240 + th * 1024;
  const u32 wdb = (u32)((kh * 43 + fh * 5) * 16384);
  const u32 csb = OFF_CS + (u32)(((xcd >> 2) * 16 + grp) * 8 * 1536);
  u32x2 sg = {0, 0}, cg[4] = {}; u32x4 uv0 = {0, 0, 0, 0}, uv1 = {0, 0, 0, 0};
  auto ldcs = [&]() { ALDW64(sg, o_st); aldw64o<0>(cg[0], o_cs, ws); aldw64o<1536>(cg[1], o_cs, ws); aldw64o<3072>(cg[2], o_cs, ws); aldw64o<-1536>(cg[3], o_cs, ws); };
  auto side = [&](auto sc, auto nc) {
    constexpr int s = decltype(sc)::value, n = decltype(nc)::value;
    if constexpr (s == 0 && n == 6) { wa[1] = wa[0] + 6 * PART; wb[1] = wb[0] + 6 * PART; PIN(wa[1]); PIN(wb[1]); }
    if constexpr (s == 1 && n < 4) { xa[1][n] = xa[0][n] + 2 * STG; PIN(xa[1][n]); }
    if constexpr (s == 1 && n == 7) {
      const float c0 = cs[0][0], c1 = cs[1][0], c2 = cs[2][0], v = tq == 0 ? c0 : (tq == 1 ? c1 : c2);
      __builtin_amdgcn_raw_buffer_store_b64(u32x2{__builtin_bit_cast(u32, v), ep}, rws, lane < 32 ? (u32)(lane * 8) : OOB, csb + dg * 1536 + (fh * 3 + (tq < 2 ? tq : 2)) * 256, SC1);
    }
    if constexpr (s == 3 && n == 6) { o_st = OFF_ST + ((panel * 4 + tq) * 32 + r32) * 8; o_cs = csb + (h * 4 + 1) * 1536 + (w < 5 ? w : 5) * 256 + r32 * 8; PIN(o_st); PIN(o_cs); }
    if constexpr (s == 4 && n == 6) { o_u = OFF_XT + (idx >> 1) * 32768 + (w * 32 + (lane >> 2)) * 128 + (lane & 2) * 32 + (idx & 1) * 32 + (lane & 1) * 16; PIN(o_u); }
    if constexpr (s == 6 && n == 2) {
#pragma unroll 1
      for (int i = 0; i < SPIN_G && !wall((sg[1] == ep) & (cg[0][1] == ep) & (cg[1][1] == ep) & (cg[2][1] == ep) & (cg[3][1] == ep)); i++) {
        __builtin_amdgcn_s_sleep(8);
        ldcs(); wait0(sg, cg);
      }
    }
    if constexpr (s == 6 && n == 6) {
      const u32 a0 = cg[0][0], a1 = cg[1][0], a2 = cg[2][0], a3 = cg[3][0];
      const float p = (__builtin_bit_cast(float, a3) + __builtin_bit_cast(float, a0)) + (__builtin_bit_cast(float, a1) + __builtin_bit_cast(float, a2));
      const auto sw = __builtin_amdgcn_permlane32_swap(__builtin_bit_cast(u32, p), __builtin_bit_cast(u32, p), false, false);
      const u32 s0 = sw[0], s1 = sw[1];
      const float v = __builtin_bit_cast(float, s0) + __builtin_bit_cast(float, s1), hi = rbf(v);
      *(LAS u32*)(sm + M_CS + (w < 5 ? w : 5) * 128 + r32 * 4) = pk2(hi, v - hi);
    }
  };
  auto stage = [&](auto sc) {
    constexpr int s = decltype(sc)::value, SP = ((s + 2) % 3) * STG;
    auto dma = [&](auto ic) {
      constexpr int i = decltype(ic)::value;
      if constexpr (s >= 6) dma16<SP + i * 2048>(ld, wdo, rd, wdb + ((s - 6) * 10 + i) * 16384);
      else if constexpr (i < 3) dma16<SP + i * 1024>(lw, wo, i ? rw2 : rw, sow(s + 2) + i * 64);
      else dma16<SP + (i - 3) * 1024>(lx, i == 3 ? xo : xo2, rx, sox(s + 2));
    };
    if (s == 5) ldcs();
    if (s == 7) { ALDW128(uv0, o_u, 0); ALDW128(uv1, o_u, 2048); }
    auto slot = [&](auto nc) {
      constexpr int n = decltype(nc)::value, k = n / 3, g = n % 3;
      acc[g] = MFMA32(W[g], X, acc[g]);
      if constexpr (s == 0) cs[g] = MFMA16(csel, W[g], cs[g]);
      if constexpr (k < 3) W[g] = rdw(s, k + 1, g);
      if constexpr (n == 0) dma(IC<0>{});
      if constexpr (n == 1) dma(IC<1>{});
      if constexpr (n == 3) dma(IC<2>{});
      if constexpr (n == 4) dma(IC<3>{});
      if constexpr (n == 5) dma(IC<4>{});
      SCHED();
      side(sc, nc);
      SCHED();
    };
    bf16x8 X1 = rdx(s, 1);
    slot(IC<0>{}); slot(IC<1>{}); slot(IC<2>{});
    X = X1; X1 = rdx(s, 2);
    slot(IC<3>{}); slot(IC<4>{}); slot(IC<5>{});
    X = X1; X1 = rdx(s, 3);
    slot(IC<6>{}); slot(IC<7>{}); slot(IC<8>{});
    X = X1;
    slot(IC<9>{});
    GST(s);
    if (s == 0) VBAR(7);
    else if (s == 1 || s == 2) VBAR(6);
    else if (s == 5) bar5(sg, cg);
    else if (s == 7) FBAR();
    else VBAR(5);
    if (s < 7) { X1 = rdx(s + 1, 0); W[0] = rdw(s + 1, 0, 0); }
    SCHED();
#pragma unroll
    for (int g = 1; g < 3; g++) { acc[g] = MFMA32(W[g], X, acc[g]); if (s == 0) cs[g] = MFMA16(csel, W[g], cs[g]); if (s < 7) W[g] = rdw(s + 1, 0, g); SCHED(); }
    X = X1;
  };
  stage(IC<0>{}); stage(IC<1>{}); stage(IC<2>{}); stage(IC<3>{}); stage(IC<4>{}); stage(IC<5>{}); stage(IC<6>{}); stage(IC<7>{});
  PST(4); GST(8);
  {
    const u32 stt = sg[0];
    const u32 nm = h ? 0u : (((stt & 0xffffu) ^ 0x8000u) * 0x10001u);
    const bf16x8 mb = __builtin_bit_cast(bf16x8, u32x4{nm, 0u, 0u, 0u});
#pragma unroll
    for (int g = 0; g < 3; g++) acc[g] = MFMA32(__builtin_bit_cast(bf16x8, u32x4{*(const LAS u32*)(sm + M_CS + (fh * 3 + g) * 128 + r32 * 4), 0u, 0u, 0u}), mb, acc[g]);
    const float sd = bfhi(stt), c1 = -1.44269504088896341f * __builtin_amdgcn_rcpf(sd), s2 = sd * sd;
    const u32 ho = (u32)(lane * 16) + (u32)(OFF_HF + ((panel * 4 + tq) * KS2 + grp * 6 + 3 * fh) * 1024);
#pragma unroll
    for (int g = 0; g < 3; g++) {
      u32x4 o;
#pragma unroll
      for (int r = 0; r < 4; r++) {
        const f32x2 gg = {acc[g][2 * r], acc[g][2 * r + 1]}, u = {acc[g][2 * r + 8], acc[g][2 * r + 9]};
        const f32x2 t = gg * f32x2{c1, c1};
        const f32x2 e = {__builtin_amdgcn_exp2f(t[0]), __builtin_amdgcn_exp2f(t[1])};
        const f32x2 dn = __builtin_elementwise_fma(e, f32x2{s2, s2}, f32x2{s2, s2});
        o[r] = pkc(gg * u * f32x2{__builtin_amdgcn_rcpf(dn[0]), __builtin_amdgcn_rcpf(dn[1])});
      }
      __builtin_amdgcn_raw_buffer_store_b128(o, rws, grp * 6 + 3 * fh + g < KS2 ? ho + g * 1024 : OOB, 0, SC1);
    }
  }
  SCHED();
  if (w < 4) {
    const LAS char* kd = sm + M_KD + kh * KDS + r32 * (KDR * 2);
    LAS char* ra = sm + CV0 + kh * CV_D + CV_RA + (255 - lane) * 16;
    const u32 SW = 0x01000302u, HL = 0x03020504u, HH = 0x03020302u;
    const int nx = r32 == 31 ? -31 * KDR * 2 : KDR * 2;
    {
      const int ro = (16 + 2 * th + h) * 2;
      const u32 v0 = *(const LAS u16*)(kd + ro), v1 = *(const LAS u16*)(kd + (r32 < 31 ? nx + ro : (ro == 38 ? KDR * 2 : nx + ro + 2)));
      const u32 p0 = PERM(v0, v0), p1 = PERM(v1, v1), p10 = PERM(v0, v1);
      *(LAS u32x4*)(ra - (128 + 64 * th) * 16) = u32x4{p1, (lane & 1) ? p10 : p0, p0, p0};
      *(LAS u32x4*)(ra - (128 + 64 * th) * 16 + (CV_RB - CV_RA)) = u32x4{p1, (lane & 1) ? p1 : p10, p0, p0};
    }
    u32x4 va, vb;
    if (th) {
      const u32 d1 = *(const LAS u32*)(kd + 24 + 4 * h), v2 = *(const LAS u16*)(kd + nx + 24 + 4 * h + (r32 == 31 ? 4 : 0));
      const u32 p0 = PERM(d1, d1), p1 = __builtin_amdgcn_perm(d1, d1, HH);
      va = u32x4{__builtin_amdgcn_perm(v2, d1, HL), p1, p0, p0}; vb = u32x4{PERM(v2, v2), p1, __builtin_amdgcn_perm(d1, d1, SW), p0};
    } else {
      const u32x2 a = *(const LAS u32x2*)(kd + 16 * h), c = *(const LAS u32x2*)(kd + 8);
      u32 d0 = *(const LAS u32*)(kd + (h ? 31 * KDR * 2 : -KDR * 2) + 12);
      const u32 v4 = *(const LAS u16*)(kd + nx + (r32 == 31 ? 24 : 16));
      d0 = lane == 0 ? 0u : d0;
      const u32 d1 = a[0], d2 = a[1], d3 = c[0], d4 = c[1], p0 = PERM(d1, d1), p2 = PERM(d2, d2);
      const u32 s1 = __builtin_amdgcn_perm(d1, d1, SW), s2 = __builtin_amdgcn_perm(d2, d2, SW), t10 = __builtin_amdgcn_perm(d1, d0, HL), t21 = __builtin_amdgcn_perm(d2, d1, HL);
      va = h ? u32x4{s2, p2, s1, lane == 32 ? t10 : p0} : u32x4{__builtin_amdgcn_perm(d4, d3, HL), __builtin_amdgcn_perm(d3, d2, HL), t21, t10};
      vb = h ? u32x4{__builtin_amdgcn_perm(v4, d2, HL), p2, t21, p0} : u32x4{__builtin_amdgcn_perm(d4, d4, SW), __builtin_amdgcn_perm(d3, d3, SW), s2, s1};
    }
    *(LAS u32x4*)(ra - 64 * th * 16) = va; *(LAS u32x4*)(ra - 64 * th * 16 + (CV_RB - CV_RA)) = vb;
    LAS char* z = sm + CV0 + kh * CV_D + lane * 8;
    *(LAS u32*)(z + th * (CV_UB + 4)) = 0u; *(LAS u32*)(z + th * (CV_UB + 4) + 4) = 0u;
    if (lane < 8) *(LAS u32x2*)(z + CV_RA + 4096 + th * (CV_RB - CV_RA)) = u32x2{0, 0};
  }
  asm volatile("s_waitcnt vmcnt(8)" : "+v"(uv0), "+v"(uv1) :: "memory");
#pragma unroll 1
  for (int i = 0; i < SPIN_G && !wall((uv0[1] == ep) & (uv0[3] == ep) & (uv1[1] == ep) & (uv1[3] == ep)); i++) {
    __builtin_amdgcn_s_sleep(8);
    ALDW128(uv0, o_u, 0); ALDW128(uv1, o_u, 2048);
    asm volatile("s_waitcnt vmcnt(0)" : "+v"(uv0), "+v"(uv1) :: "memory");
  }
  {
    LAS char* ud = sm + CV0 + (lane & 1) * CV_D + (w * 32 + (lane >> 2)) * 16 + (lane & 2) * 4;
    *(LAS u32x2*)(ud + CV_UA + 512) = u32x2{uv0[0], uv0[2]}; *(LAS u32x2*)(ud + CV_UA + 768) = u32x2{uv1[0], uv1[2]};
    *(LAS u32*)(ud + CV_UB + 516) = uv0[0]; *(LAS u32*)(ud + CV_UB + 520) = uv0[2]; *(LAS u32*)(ud + CV_UB + 772) = uv1[0]; *(LAS u32*)(ud + CV_UB + 776) = uv1[2];
  }
  PST(5);
  LAS char* cc = sm + CV0 + kh * CV_D;
  const int T = tb * 2 + th;
  const u32 f2p = lane == 0 ? OFF_F2 + ((panel * 4 + tq) * 32 + grp * 2 + fh) * 128 : OOB;
  if (w >= 4) {
    SCHED(); VBAR(3); SCHED();
    PST(6);
    const u32 l2 = (u32)(size_t)sm + STG + kh * 20480 + th * 1024, s2 = (u32)((kh * 43 + 20) * 16384);
    dma16<0>(l2, wdo, rd, s2); dma16<2048>(l2, wdo, rd, s2 + 16384); dma16<4096>(l2, wdo, rd, s2 + 2 * 16384); dma16<6144>(l2, wdo, rd, s2 + 3 * 16384);
    WAITV(4);
    __builtin_amdgcn_raw_buffer_store_b64(tag, rws, f2p, 0, SC1);
    dma16<8192>(l2, wdo, rd, s2 + 4 * 16384); dma16<10240>(l2, wdo, rd, s2 + 5 * 16384); dma16<12288>(l2, wdo, rd, s2 + 6 * 16384);
    dma16<14336>(l2, wdo, rd, s2 + 7 * 16384); dma16<16384>(l2, wdo, rd, s2 + 8 * 16384); dma16<18432>(l2, wdo, rd, s2 + 9 * 16384);
    u32x4 wpark[13];
    auto rdd = __builtin_amdgcn_make_buffer_rsrc((void*)wd, 0, F * D * 2, 0x00020000);
#pragma unroll
    for (int k = 0; k < 13; k++) wpark[k] = __builtin_amdgcn_raw_buffer_load_b128(rdd, wdo, s2 + (10 + k) * 16384, 0);
    PST(7);
    const u32 f2o = OFF_F2 + (T * 32 + kh * 14 + c16) * 128;
    u32x2 f2 = {0, 0};
#pragma unroll 1
    for (int i = 0; i < SPIN_G; i++) {
      ALDW64(f2, f2o);
      asm volatile("s_waitcnt vmcnt(0)" : "+v"(f2) :: "memory");
      if (wall(TAGOK(f2))) break;
    }
    PST(8);
    constexpr int HP = 12;
    u32x4 Hf[HP];
    const u32 hvo = OFF_HF + (T * KS2 + kh * 43) * 1024 + lane * 16;
    auto ldh = [&](int i) { return __builtin_amdgcn_raw_buffer_load_b128(rws, hvo + (u32)(i & 3) * 1024u, (u32)((i & ~3) * 1024), SC1); };
#pragma unroll
    for (int i = 0; i < HP; i++) Hf[i] = ldh(i);
    const int trow = T * 32 + r32, dcol = dg * 64 + kh * 32 + h * 4;
    auto aoff = [&](int i) { int k = i >= 30 ? i - 30 : i; return ((k / 10 + 2) % 3) * STG + (k % 10) * 2048; };
    const LAS char* ar0 = sm + kh * 20480 + (4 * h + q4) * 128 + (((q & 1) * 32 + (lane & 3) * 8) ^ ((q4 >> 1) * 64));
    const LAS char* ard[2][2] = {{ar0, (const LAS char*)((size_t)ar0 ^ 64)}, {ar0 + 2 * STG, (const LAS char*)((size_t)ar0 ^ 64) + 2 * STG}};
    LAS char* pk[2] = {sm + kh * 20480 + th * 1024 + lane * 16, sm + 2 * STG + kh * 20480 + th * 1024 + lane * 16};
    PIN(ard[1][0]); PIN(ard[1][1]); PIN(pk[1]);
    auto rda = [&](int i, int dt) { const int o = aoff(i); return o >= 2 * STG ? trfrag<1024>(ard[1][dt] + (o - 2 * STG)) : trfrag<1024>(ard[0][dt] + o); };
    f32x16 a0 = {}, a1 = {};
    bf16x8 A[3][2] = {{rda(0, 0), rda(0, 1)}, {rda(1, 0), rda(1, 1)}, {}};
    u32x2 xr[4];
#pragma unroll
    for (int i = 0; i < 43; i++) {
      if (i == 28) lds_wait<1>(sm, M_P + (w ^ 1) * 4, 2);
      if (i + 2 < 43) { A[(i + 2) % 3][0] = rda(i + 2, 0); A[(i + 2) % 3][1] = rda(i + 2, 1); }
      a0 = MFMA32(A[i % 3][0], __builtin_bit_cast(bf16x8, Hf[i % HP]), a0);
      a1 = MFMA32(A[i % 3][1], __builtin_bit_cast(bf16x8, Hf[i % HP]), a1);
      if (i + HP < 43) Hf[i % HP] = ldh(i + HP);
      if (i == 10) lds_post(sm, M_P + w * 4, 1);
      if (i == 12) lds_wait<1>(sm, M_P + (w ^ 1) * 4, 1);
      if (i >= 13 && i < 26) { const int o = aoff(i - 13); *(LAS u32x4*)(o >= 2 * STG ? pk[1] + (o - 2 * STG) : pk[0] + o) = wpark[i - 13]; }
      if (i == 25) lds_post(sm, M_P + w * 4, 2);
      if (i >= 31 && i < 35) xr[i - 31] = *(const u32x2*)(x + (size_t)trow * D + dcol + (i - 31) * 8);
      SCHED();
    }
    PST(9);
    asm volatile("" : "+v"(a0), "+v"(a1));
    {
      f32x16 snd = kh ? a0 : a1;
      LAS char* rb = sm + STG + kh * 20480 + th * 4096 + lane * 16;
#pragma unroll
      for (int g = 0; g < 4; g++) *(LAS f32x4*)(rb + g * 1024) = f32x4{snd[4 * g], snd[4 * g + 1], snd[4 * g + 2], snd[4 * g + 3]};
    }
    f32x16 kp = kh ? a1 : a0;
    FBAR();
    PST(10);
    {
      const LAS char* rb = sm + STG + (kh ^ 1) * 20480 + th * 4096 + lane * 16;
      const LAS char* yb = cc + th * 2048 + (4 * h + q4) * 64 + (q & 1) * 32 + (lane & 3) * 8;
      u32x2 o[4];
#pragma unroll
      for (int g = 0; g < 4; g++) {
        f32x4 vv = *(const LAS f32x4*)(rb + g * 1024);
        u32x2 y = trread(yb + g * 512);
        u32 f0 = pk2(vv[0] + kp[4 * g], vv[1] + kp[4 * g + 1]), f1 = pk2(vv[2] + kp[4 * g + 2], vv[3] + kp[4 * g + 3]);
        u32 r0 = pk2(bflo(xr[g][0]) + bflo(y[0]), bfhi(xr[g][0]) + bfhi(y[0]));
        u32 r1 = pk2(bflo(xr[g][1]) + bflo(y[1]), bfhi(xr[g][1]) + bfhi(y[1]));
        o[g] = u32x2{pk2(bflo(r0) + bflo(f0), bfhi(r0) + bfhi(f0)), pk2(bflo(r1) + bflo(f1), bfhi(r1) + bfhi(f1))};
      }
#pragma unroll
      for (int g = 0; g < 4; g += 2) {
        auto s0 = __builtin_amdgcn_permlane32_swap(o[g][0], o[g + 1][0], false, false);
        auto s1 = __builtin_amdgcn_permlane32_swap(o[g][1], o[g + 1][1], false, false);
        *(u32x4*)(out + (size_t)trow * D + dg * 64 + kh * 32 + g * 8 + h * 8) = u32x4{s0[0], s1[0], s0[1], s1[1]};
      }
    }
  } else {
    WAITV(0);
    __builtin_amdgcn_raw_buffer_store_b64(tag, rws, f2p, 0, SC1);
    const LAS char* ub = cc + ((c16 & 1) ? CV_UA : CV_UB) + 2 * (256 + 32 * (c16 >> 1) + 8 * q) - th * 256 - 192;
    const LAS char* tbp = cc + ((c16 & 1) ? CV_RB : CV_RA) + 2 * (2044 - 4 * (c16 >> 1) + 8 * q) - th * 256 - 7 * 512 - 192;
    PIN(ub); PIN(tbp);
    SCHED(); FBAR(); SCHED();
    PST(6);
    f32x4 ya[8] = {};
    conv_main(ub, tbp, ya);
    PST(7);
    lds_post(sm, M_P + w * 4, 1);
    const int toff = 8 * q + 2 * (c16 & 1) + 32 * (c16 >> 1);
    u32 u2[4][2];
#pragma unroll
    for (int a = 0; a < 4; a++)
#pragma unroll
      for (int e = 0; e < 2; e++) u2[a][e] = *(const LAS u32*)(cc + CV_UA + 2 * (256 + 256 * (4 * th + a) + toff + 4 * e));
    lds_wait<1>(sm, M_P + (w ^ 1) * 4, 1);
#pragma unroll
    for (int a = 0; a < 4; a++) {
      f32x4 lo = ya[a], hi = ya[4 + a];
      asm volatile("" : "+v"(lo), "+v"(hi));
      *(LAS f32x4*)(cc + CV_RA + th * 4096 + a * 1024 + lane * 16) = th ? lo : hi;
      ya[a] = th ? hi : lo;
    }
    lds_post(sm, M_P + w * 4, 2);
    const float dskip = bflo(p_ds);
    u32 sk[4][2];
#pragma unroll
    for (int a = 0; a < 4; a++)
#pragma unroll
      for (int e = 0; e < 2; e++) sk[a][e] = pk2(bflo(u2[a][e]) * dskip, bfhi(u2[a][e]) * dskip);
    lds_wait<1>(sm, M_P + (w ^ 1) * 4, 2);
    u32 p[4][2];
#pragma unroll
    for (int a = 0; a < 4; a++) {
      f32x4 yv = ya[a] + *(const LAS f32x4*)(cc + CV_RA + (th ^ 1) * 4096 + a * 1024 + lane * 16);
#pragma unroll
      for (int e = 0; e < 2; e++) {
        const u32 yb = pk2(yv[2 * e], yv[2 * e + 1]);
        const u32 pv = pk2(bflo(yb) + bflo(sk[a][e]), bfhi(yb) + bfhi(sk[a][e]));
        p[a][e] = pv < 0xfffffffeu ? pv : 0xfffffffeu;
      }
    }
#pragma unroll
    for (int a = 0; a < 4; a += 2) {
      const bool odd = c16 & 1;
      u32 s0 = odd ? p[a][0] : p[a + 1][0], s1 = odd ? p[a][1] : p[a + 1][1];
      u32 r0 = __builtin_amdgcn_mov_dpp(s0, 0xB1, 0xf, 0xf, true), r1 = __builtin_amdgcn_mov_dpp(s1, 0xB1, 0xf, 0xf, true);
      u32x4 o = {odd ? r0 : p[a][0], odd ? p[a + 1][0] : r0, odd ? r1 : p[a][1], odd ? p[a + 1][1] : r1};
      const int t = 256 * (4 * th + a + (c16 & 1)) + 8 * q + 32 * (c16 >> 1);
      __builtin_amdgcn_raw_buffer_store_b128(o, rws, (u32)(t * 2) + (u32)(OFF_YT + (2 * idx + kh) * L * 2), 0, SC1);
    }
    PST(8);
    if (b == 0 && tid == 0) __builtin_amdgcn_raw_buffer_store_b32(ep, rws, 0, OFF_EP, SC1);
    const u32 yo = OFF_YT + ((dg * 64 + kh * 32 + (lane >> 2)) * L + T * 32 + (lane & 3) * 8) * 2;
    u32x4 y0, y1;
#pragma unroll 1
    for (int i = 0; i < SPIN_G; i++) {
      __builtin_amdgcn_s_sleep(16);
      y0 = __builtin_amdgcn_raw_buffer_load_b128(rws, yo, 0, SC1); y1 = __builtin_amdgcn_raw_buffer_load_b128(rws, yo, 16 * L * 2, SC1);
      const u32x4 m = __builtin_elementwise_max(y0, y1);
      const u32 m0 = m[0] > m[1] ? m[0] : m[1], m1 = m[2] > m[3] ? m[2] : m[3];
      if (wall((m0 > m1 ? m0 : m1) != ~0u)) break;
      asm volatile("" ::: "memory");
    }
    *(LAS u32x4*)(cc + th * 2048 + lane * 16) = y0;
    *(LAS u32x4*)(cc + th * 2048 + 1024 + lane * 16) = y1;
    PST(9);
    FBAR();
  }
}

extern "C" void kernel_launch(void* const* d_in, const int* in_sizes, int n_in, void* d_out, int out_size, void* d_ws, size_t ws_size, hipStream_t stream) {
  const u16* const* in = (const u16* const*)d_in;
  k_fused<<<256, 512, 0, stream>>>(in[0], in[1], in[2], in[3], in[4], in[5], in[6], in[7], in[8], in[10], in[11], in[12], (char*)d_ws, (u16*)d_out);
}
```

```cpp
#include <hip/hip_runtime.h>
#include <hip/hip_bf16.h>
#include <stdint.h>

typedef unsigned short u16;
typedef unsigned int u32;
typedef __attribute__((ext_vector_type(8))) short bf16x8;
typedef __attribute__((ext_vector_type(4))) short s16x4;
typedef __attribute__((ext_vector_type(4))) float f32x4;
typedef __attribute__((ext_vector_type(16))) float f32x16;
typedef __attribute__((ext_vector_type(4))) u32 u32x4;
typedef __attribute__((ext_vector_type(2))) u32 u32x2;
typedef __attribute__((ext_vector_type(2))) float f32x2;
typedef __attribute__((ext_vector_type(2))) __bf16 bf2_t;
#define LAS __attribute__((address_space(3)))
#define DEVI __device__ __forceinline__

constexpr int L = 2048, D = 512, NS = 128, F = 1376;
constexpr int KS2 = 86;
constexpr u32 OFF_XT = 0;
constexpr u32 OFF_YT = 4u << 20;
constexpr u32 OFF_HF = 6u << 20;
constexpr u32 OFF_ST = 12u << 20;
constexpr u32 OFF_CS = OFF_ST + 65536;
constexpr u32 OFF_EP = OFF_CS + 32 * 8 * 192 * 8;
constexpr u32 OFF_F2 = OFF_EP + 4096;
constexpr u32 WS_USED = 13u << 20;

DEVI float bflo(u32 v) { return __builtin_bit_cast(float, v << 16); }
DEVI float bfhi(u32 v) { return __builtin_bit_cast(float, v & 0xffff0000u); }
DEVI u32 pk2(float lo, float hi) { f32x2 f = {lo, hi}; bf2_t b = __builtin_convertvector(f, bf2_t); return __builtin_bit_cast(u32, b); }
DEVI float rbf(float f) { return bflo(pk2(f, f)); }
DEVI bool wall(bool ok) { return __builtin_amdgcn_ballot_w64(!ok) == 0ull; }
#define WAITV(n) asm volatile("s_waitcnt vmcnt(" #n ")" ::: "memory")
#define FBAR() asm volatile("s_waitcnt lgkmcnt(0)\n\ts_barrier" ::: "memory")
#define VBAR(n) asm volatile("s_waitcnt vmcnt(" #n ") lgkmcnt(0)\n\ts_barrier" ::: "memory")
#define SCHED() __builtin_amdgcn_sched_barrier(0)
#define PIN(v) asm volatile("" : "+v"(v))
#define MFMA32(a, b, c) __builtin_amdgcn_mfma_f32_32x32x16_bf16(a, b, c, 0, 0, 0)
#define MFMA16(a, b, c) __builtin_amdgcn_mfma_f32_16x16x32_bf16(a, b, c, 0, 0, 0)
#define PERM(hi, lo) __builtin_amdgcn_perm(hi, lo, 0x05040100u)
#define SC1 16
#define OOB 0x80000000u
#define WROT ((tb & 3) * 11)
#define ROT dg
#define GST(s)
#define PST(i) STAMP(i)
template <int N> struct IC { static constexpr int value = N; };

#define DIAG_DECL
#define STAMP(i)

typedef f32x2 cf;
DEVI cf cmul(cf b, cf a) { const cf t = cf{a[0], a[0]} * b; return __builtin_elementwise_fma(cf{-a[1], a[1]}, cf{b[1], b[0]}, t); }
DEVI u32 pkc(cf v) { return __builtin_bit_cast(u32, __builtin_convertvector(v, bf2_t)); }
DEVI cf cexp2_(float lr, float rev, float x) {
  float e = __builtin_amdgcn_exp2f(lr * x), f = __builtin_amdgcn_fractf(rev * x);
  return cf{e, e} * cf{__builtin_amdgcn_cosf(f), __builtin_amdgcn_sinf(f)};
}
DEVI u32x4 mkrsrc(const void* p, u32 bytes) { u32x4 r = {(u32)(size_t)p, (u32)((size_t)p >> 32) & 0xffffu, bytes, 0x00020000u}; return r; }
template <int IMM>
DEVI void dma16(u32 lds, u32 voff, u32x4 rsrc, u32 soff) {
  asm volatile("s_add_u32 m0, %0, %4\n\ts_nop 0\n\tbuffer_load_dwordx4 %1, %2, %3 offen lds" :: "s"(lds), "v"(voff), "s"(rsrc), "s"(soff), "i"(IMM) : "scc");
}
DEVI u32x2 trread(const LAS char* p) { return __builtin_bit_cast(u32x2, __builtin_amdgcn_ds_read_tr16_b64_v4i16((LAS s16x4*)p)); }
template <int STEP>
DEVI bf16x8 trfrag(const LAS char* p) {
  s16x4 lo = __builtin_amdgcn_ds_read_tr16_b64_v4i16((LAS s16x4*)p), hi = __builtin_amdgcn_ds_read_tr16_b64_v4i16((LAS s16x4*)(p + STEP));
  return __builtin_shufflevector(lo, hi, 0, 1, 2, 3, 4, 5, 6, 7);
}
template <int CTRL, int RM> DEVI float dppadd(float v) { return v + __builtin_bit_cast(float, __builtin_amdgcn_update_dpp(0, __builtin_bit_cast(int, v), CTRL, RM, 0xf, true)); }
DEVI float lane63(float v) { return __builtin_bit_cast(float, __builtin_amdgcn_readlane(__builtin_bit_cast(int, v), 63)); }
DEVI float dot2(u32 a, u32 b, float c) { return __builtin_amdgcn_fdot2_f32_bf16(__builtin_bit_cast(bf2_t, a), __builtin_bit_cast(bf2_t, b), c, false); }

constexpr int PART = 10368, P_G = 4096, P_U = 7200;
constexpr int STG = 4 * PART;
constexpr int CV0 = 3 * STG;
constexpr int CV_UA = 0, CV_UB = 4640, CV_RA = 9344, CV_RB = 13568, CV_D = 17920;
constexpr int KX = 5888, KY = 8704, CV_LN = 2 * KX;
constexpr int MISC = CV0 + 2 * CV_D;
constexpr int M_P = MISC;
constexpr int M_KD = MISC + 48;
constexpr int KDR = 20, KDS = 32 * KDR * 2 + 16;
constexpr int M_CS = M_KD + 2 * KDS;
constexpr int LDS_BYTES = M_CS + 768;
constexpr u32 CODE_BYTES = 21 * 1024;
constexpr int SPIN_L = 1 << 18, SPIN_G = 1 << 16;

DEVI void lds_post(LAS char* sm, int word_off, u32 v) { if ((threadIdx.x & 63) == 0) *(volatile LAS u32*)(sm + word_off) = v; }
template <int N>
DEVI void lds_wait(LAS char* sm, int off, u32 v) {
#pragma unroll 1
  for (int i = 0; i < SPIN_L; i++) {
    u32 m;
    if (N == 1) m = *(volatile LAS u32*)(sm + off);
    else if (N == 2) { u32x2 p = *(volatile LAS u32x2*)(sm + off); m = p[0] < p[1] ? p[0] : p[1]; }
    else { u32x4 p = *(volatile LAS u32x4*)(sm + off); u32 a = p[0] < p[1] ? p[0] : p[1], b = p[2] < p[3] ? p[2] : p[3]; m = a < b ? a : b; }
    if ((u32)__builtin_amdgcn_readfirstlane(m) >= v) break;
    __builtin_amdgcn_s_sleep(1);
  }
  asm volatile("" ::: "memory");
}
#define ALD16(dst, base, voff) asm volatile("global_load_ushort %0, %1, %2" : "=v"(dst) : "v"(voff), "s"(base))
#define ALD128(dst, base, voff) asm volatile("global_load_dwordx4 %0, %1, %2" : "=v"(dst) : "v"(voff), "s"(base))
DEVI void aldw64(u32x2& dst, u32 voff, const char* base) { asm volatile("global_load_dwordx2 %0, %1, %2 sc1" : "+v"(dst) : "v"(voff), "s"(base)); }
template <int IMM> DEVI void aldw128(u32x4& dst, u32 voff, const char* base) { asm volatile("global_load_dwordx4 %0, %1, %2 offset:%3 sc1" : "+v"(dst) : "v"(voff), "s"(base), "i"(IMM)); }
template <int IMM> DEVI void aldw64o(u32x2& dst, u32 voff, const char* base) { asm volatile("global_load_dwordx2 %0, %1, %2 offset:%3 sc1" : "+v"(dst) : "v"(voff), "s"(base), "i"(IMM)); }
DEVI void wait0(u32x2& a, u32x2 (&c)[4]) { asm volatile("s_waitcnt vmcnt(0)" : "+v"(a), "+v"(c[0]), "+v"(c[1]), "+v"(c[2]), "+v"(c[3]) :: "memory"); }
DEVI void wait5(u32x2& a, u32x2 (&c)[4]) { asm volatile("s_waitcnt vmcnt(5)" : "+v"(a), "+v"(c[0]), "+v"(c[1]), "+v"(c[2]), "+v"(c[3]) :: "memory"); }
DEVI void aldpark(u32x4& dst, u32 voff, u32x4 rsrc, u32 soff) { asm volatile("buffer_load_dwordx4 %0, %1, %2, %3 offen" : "+v"(dst) : "v"(voff), "s"(rsrc), "s"(soff)); }
#define ALDW64(dst, voff) aldw64(dst, voff, ws)
#define ALDW128(dst, voff, imm) aldw128<imm>(dst, voff, ws)

template <class HOOK>
DEVI void conv_main(const LAS char* ub, const LAS char* tb, f32x4 (&acc)[8], HOOK&& hook) {
  auto rdU = [&](int r, int n) { return *(const LAS bf16x8*)(ub + (3 - r) * 64 + n * 512); };
  auto rdT = [&](int r, int n) {
    s16x4 lo = *(const volatile LAS s16x4*)(tb + (3 - r) * 64 + (7 - n) * 512), hi = *(const volatile LAS s16x4*)(tb + (3 - r) * 64 + (7 - n) * 512 + 8);
    return (bf16x8)__builtin_shufflevector(lo, hi, 0, 1, 2, 3, 4, 5, 6, 7);
  };
  bf16x8 U[2][8], T[2][8];
  U[0][0] = rdU(0, 0);
#pragma unroll
  for (int n = 0; n < 8; n++) T[0][n] = rdT(0, n);
#pragma unroll
  for (int n = 1; n < 8; n++) U[0][n] = rdU(0, n);
#pragma unroll
  for (int r = 0; r < 4; r++) {
    const int p = r & 1;
    int k = 0;
#pragma unroll
    for (int n = 0; n < 8; n++)
#pragma unroll
      for (int t = 0; t + n < 8; t++) {
        acc[t + n] = MFMA16(T[p][t], U[p][n], acc[t + n]);
        if (r < 3 && k < 32 && (k & 1)) { if (k < 16) T[p ^ 1][k >> 1] = rdT(r + 1, k >> 1); else U[p ^ 1][(k >> 1) - 8] = rdU(r + 1, (k >> 1) - 8); }
        hook(r * 36 + k);
        k++;
        SCHED();
      }
  }
}

__global__ __launch_bounds__(512, 2) void k_fused(const u16* __restrict__ x, const u16* __restrict__ Are, const u16* __restrict__ Aim,
    const u16* __restrict__ Bre, const u16* __restrict__ Bim, const u16* __restrict__ Cre, const u16* __restrict__ Cim,
    const u16* __restrict__ ldt, const u16* __restrict__ dsk, const u16* __restrict__ wg, const u16* __restrict__ wu,
    const u16* __restrict__ wd, char* ws, u16* __restrict__ out) {
  __shared__ __attribute__((aligned(16))) char smem[LDS_BYTES];
  LAS char* sm = (LAS char*)smem;
  const int b = blockIdx.x, tid = threadIdx.x, lane = tid & 63, w = __builtin_amdgcn_readfirstlane(tid >> 6);
  const int xcd = b & 7, j = b >> 3;
  DIAG_DECL STAMP(0);
  const int panel = (xcd >> 2) * 8 + (j & 7), grp = (xcd & 3) * 4 + (j >> 3);
  const int tb = 4 * xcd + (j >> 3), dg = j & 7;
  const int idx = 32 * xcd + j;
  const int tq = w & 3, fh = w >> 2;
  const int r32 = lane & 31, h = lane >> 5, c16 = lane & 15, q = lane >> 4, q4 = c16 >> 2;
  asm volatile("" :: "s"(wg), "s"(wu), "s"(wd), "s"(x), "s"(ws), "s"(dsk), "s"(out));
  u32 ep;
  asm volatile("s_load_dword %0, %1, 0x0" : "=s"(ep) : "s"(ws + OFF_EP));
  u32x4 xv; u32 p_ar, p_ai, p_br, p_bi, p_cr, p_ci, p_dt, p_ds;
  {
    const int dt_ = 2 * idx + (tq >> 1);
    const u32 g2 = (u32)(dt_ * NS + (w & 1) * 64 + lane) * 2u, d2 = (u32)dt_ * 2u;
    ALD16(p_ar, Are, g2); ALD16(p_ai, Aim, g2); ALD16(p_dt, ldt, d2);
    ALD128(xv, x, (u32)((idx * 8 + w) * 1024 + lane * 16));
    ALD16(p_br, Bre, g2); ALD16(p_bi, Bim, g2); ALD16(p_cr, Cre, g2); ALD16(p_ci, Cim, g2); ALD16(p_ds, dsk, (u32)(4 * idx + 2 * fh));
  }
  {
    const u32 co = tid < 12 ? (u32)((j & 15) * 12 + tid) * 128u : 0u;
    const char* pc = (const char*)__builtin_amdgcn_s_getpc();
    asm volatile("s_mov_b32 m0, %0\n\ts_nop 0\n\tglobal_load_lds_dword %1, %2" :: "s"((u32)(size_t)sm + M_CS), "v"(co < CODE_BYTES ? co : 0u), "s"(pc));
  }
  SCHED(); __builtin_amdgcn_s_barrier(); SCHED();
  auto rws = __builtin_amdgcn_make_buffer_rsrc(ws, 0, WS_USED, 0x00020000);
  __builtin_amdgcn_raw_buffer_store_b128(u32x4{~0u, ~0u, ~0u, ~0u}, rws, (u32)(OFF_YT + idx * 8192 + tid * 16), 0, SC1);
  SCHED();
  const u32x4 rw = mkrsrc(fh ? wu : wg, grp < 15 ? D * F * 2 : 0), rw2 = mkrsrc(fh ? wu : wg, grp < 14 ? D * F * 2 : 0);
  const u32 wo = (u32)((tq * 16 + (lane >> 2)) * (F * 2) + grp * 192 + (lane & 3) * 16);
  const u32 lx = (u32)(size_t)sm + tq * PART + fh * 2048, lw = (u32)(size_t)sm + tq * PART + (fh ? P_U : P_G);
  auto sow = [&](int st) { return (u32)(((st + ROT) & 7) * (64 * F * 2)); };
  dma16<0>(lw, wo, rw, sow(0)); dma16<1024>(lw, wo, rw2, sow(0) + 64); dma16<2048>(lw, wo, rw2, sow(0) + 128);
  SCHED();
  const u32x4 rx = mkrsrc(grp < 15 ? x : wd, grp < 15 ? L * D * 2 : F * D * 2);
  u32 xo = (u32)(((panel * 4 + tq) * 32 + fh * 16 + (lane >> 3)) * 1024 + (((lane & 7) ^ q) * 16)), xo2 = (xo ^ 64u) + 8192u;
  auto sox = [&](int st) { return (u32)(((st + ROT) & 7) * 128); };
  const int th = w & 1, kh = tq >> 1;
  u32 wdo = (u32)((th * 8 + (lane >> 3)) * 1024 + dg * 128 + (((lane & 7) ^ ((q & 1) << 2)) * 16));
  PIN(xo); PIN(xo2); PIN(wdo);
  auto pdma = [&](int i) {
    SCHED();
    if (i == 0) dma16<0>(lx, xo, rx, sox(0));
    if (i == 1) dma16<1024>(lx, xo2, rx, sox(0));
    if (i == 2) dma16<STG>(lw, wo, rw, sow(1));
    if (i == 3) dma16<STG + 1024>(lw, wo, rw2, sow(1) + 64);
    if (i == 4) dma16<STG + 2048>(lw, wo, rw2, sow(1) + 128);
    if (i == 5) dma16<STG>(lx, xo, rx, sox(1));
    if (i == 6) dma16<STG + 1024>(lx, xo2, rx, sox(1));
    SCHED();
  };
  LAS char* cb = sm + CV0 + fh * CV_D;
  const LAS char* xa[2][4]; const LAS char* wa[2][3];
  xa[0][0] = sm + tq * PART + r32 * 128 + ((h ^ ((r32 >> 1) & 7)) * 16);
  const LAS char* wa0 = sm + ((q & 1) ? P_U : P_G) + (8 * h + q4) * 64 + (lane & 3) * 8;
  const int fo = c16 * 272 + h * 16;
  const LAS char* fxa = cb + (tq >> 1) * 16 * 272 + (q & 1) * KX + fo;
  const LAS char* fya = sm + 2 * STG + fh * 2 * KY + (tq & 1) * 16 * 272 + (q & 1) * KY + fo;
  const LAS char* xta = sm + CV0 + (q & 1) * CV_D + CV_LN + q4 * 1040 + (w * 64 + h * 16 + (lane & 3) * 4) * 2;
  u32 xto = OFF_XT + (u32)((w * 16 + h * 4 + q4) * 32768 + idx * 128 + (q & 1) * 64 + (lane & 3) * 16);
  PIN(xa[0][0]); PIN(wa0); PIN(fxa); PIN(fya); PIN(xta); PIN(xto);
  asm volatile("s_waitcnt vmcnt(5) lgkmcnt(0)" : "+s"(ep), "+v"(xv), "+v"(p_ar), "+v"(p_ai), "+v"(p_dt), "+v"(p_br), "+v"(p_bi), "+v"(p_cr), "+v"(p_ci), "+v"(p_ds));
  ep += 1u;
  STAMP(1);
#define tag (u32x2{ep, ep ^ 0x5bd1e995u})
#define TAGOK(v) ((bool)(((v)[0] == ep) & ((v)[1] == (ep ^ 0x5bd1e995u))))
  {
    float s = 0.f, qq = 0.f;
#pragma unroll
    for (int k = 0; k < 4; k++) { const u32 v = xv[k]; s = dot2(v, 0x3f803f80u, s); qq = dot2(v, v, qq); }
    s = dppadd<0x111, 0xf>(s); qq = dppadd<0x111, 0xf>(qq); s = dppadd<0x112, 0xf>(s); qq = dppadd<0x112, 0xf>(qq);
    s = dppadd<0x114, 0xf>(s); qq = dppadd<0x114, 0xf>(qq); s = dppadd<0x118, 0xf>(s); qq = dppadd<0x118, 0xf>(qq);
    s = dppadd<0x142, 0xa>(s); qq = dppadd<0x142, 0xa>(qq); s = dppadd<0x143, 0xc>(s); qq = dppadd<0x143, 0xc>(qq);
    const float m = s * (1.f / D), var = __builtin_fmaxf(__builtin_fmaf(-m, m, qq * (1.f / D)), 0.f);
    const u32 mv = pk2(m, var);
    const float sd = rbf(__builtin_amdgcn_sqrtf(rbf(bfhi(mv) + 1e-5f))), rs_ = __builtin_amdgcn_rcpf(sd);
    const float rs = lane63(rs_), nb = lane63(-bflo(mv) * rs_);
    u32x4 o;
#pragma unroll
    for (int k = 0; k < 4; k++) { const u32 v = xv[k]; o[k] = pk2(__builtin_fmaf(bflo(v), rs, nb), __builtin_fmaf(bfhi(v), rs, nb)); }
    *(LAS u32x4*)(cb + CV_LN + tq * 1040 + lane * 16) = o;
    if (lane == 63) { __builtin_amdgcn_raw_buffer_store_b64(u32x2{PERM(__builtin_bit_cast(u32, sd) >> 16, mv), ep}, rws, 0, OFF_ST + (idx * 8 + w) * 8, SC1); *(LAS u32*)(sm + M_P + w * 4) = 0u; }
  }
  {
    const float dt = rbf(__builtin_amdgcn_exp2f(bflo(p_dt) * 1.44269504088896341f));
    const float lr = dt * bflo(p_ar) * 1.44269504088896341f, rev = dt * bflo(p_ai) * 0.15915494309189535f;
    if (fh == 0) {
      LAS char* ty = sm + 2 * STG + tq * KY + lane * 4;
      cf Y[16];
      Y[0] = cf{1.f, 0.f}; Y[1] = cexp2_(lr, rev, 8.f);
#pragma unroll
      for (int hb = 1; hb < 16; hb <<= 1) {
        if (hb > 1) Y[hb] = cmul(Y[hb >> 1], Y[hb >> 1]);
#pragma unroll
        for (int c = 1; c < hb; c++) Y[hb + c] = cmul(Y[c], Y[hb]);
      }
      const cf y16 = cmul(Y[8], Y[8]);
#pragma unroll
      for (int c = 0; c < 16; c++) {
        cf t = c ? cmul(Y[c], y16) : y16;
        *(LAS u32*)(ty + c * 272) = pkc(Y[c]);
        *(LAS u32*)(ty + (16 + c) * 272) = pkc(t);
        if (c == 3) pdma(0);
        if (c == 11) pdma(1);
      }
    } else {
      LAS char* tx = sm + CV0 + (tq >> 1) * CV_D + (w & 1) * KX + lane * 4;
      auto put = [&](int r, cf t) { *(LAS u32*)(tx + r * 272) = pkc(t); };
      const cf Bc = {dt * bflo(p_br), -dt * bflo(p_bi)}, Cc = {bflo(p_cr), -bflo(p_ci)};
      cf X[21];
      X[0] = cmul(Cc, Bc);
      cf e1 = cexp2_(lr, -rev, 1.f), e2 = cmul(e1, e1), e4 = cmul(e2, e2), e256 = cexp2_(lr, -rev, 256.f);
      X[1] = cmul(X[0], e1); X[2] = cmul(X[0], e2); X[3] = cmul(X[1], e2);
      X[4] = cmul(X[0], e4); X[5] = cmul(X[1], e4); X[6] = cmul(X[2], e4); X[7] = cmul(X[3], e4);
#pragma unroll
      for (int r = 0; r < 8; r++) put(r, X[r]);
      pdma(0);
      X[8] = cmul(X[0], e256); X[9] = cmul(X[2], e256); X[10] = cmul(X[4], e256); X[11] = cmul(X[6], e256);
      X[12] = cmul(X[8], e256); X[13] = cmul(X[10], e256); X[14] = cmul(X[12], e256); X[15] = cmul(X[13], e256);
#pragma unroll
      for (int r = 8; r < 16; r++) put(r, X[r]);
      pdma(1);
      X[16] = cmul(X[14], e256); X[17] = cmul(X[16], e256); X[18] = cmul(X[17], e256); X[19] = cmul(X[18], e256); X[20] = cmul(X[19], e256);
#pragma unroll
      for (int r = 16; r < 21; r++) put(r, X[r]);
    }
  }
  SCHED(); VBAR(6); SCHED();
  STAMP(2);
  const u32x4 rd = mkrsrc(wd, F * D * 2);
  const u32x2 xt0 = trread(xta), xt1 = trread(xta + 64);
  {
    bf16x8 fx[8], fy[8];
#pragma unroll
    for (int k = 0; k < 8; k++) { fx[k] = *(const LAS bf16x8*)(fxa + k * 32); fy[k] = *(const LAS bf16x8*)(fya + k * 32); }
    pdma(2); pdma(3);
    f32x4 ka = {};
#pragma unroll
    for (int k = 0; k < 8; k++) ka = MFMA16(fx[k], fy[k], ka);
    ka[0] += (tq == 0 && lane == 0) ? bflo(p_ds) : 0.f;
    const int kc = c16 + 16 * (tq & 1), kr = (tq >> 1) * 16 + 4 * q;
    if (kr < 20 || (kr == 20 && kc == 0)) *(LAS u32x2*)(sm + M_KD + fh * KDS + (kr < 20 ? kc * (KDR * 2) + kr * 2 : 32 * KDR * 2)) = u32x2{pk2(ka[0], ka[1]), pk2(ka[2], ka[3])};
  }
#pragma unroll
  for (int k = 1; k < 4; k++) xa[0][k] = (const LAS char*)((size_t)xa[0][0] ^ (size_t)(k * 32));
  int tl[3];
#pragma unroll
  for (int g = 0; g < 3; g++) { const int r = g + (tq == 3 ? 0 : tq); tl[g] = fh * 3 + (r >= 3 ? r - 3 : r); wa[0][g] = wa0 + (tl[g] >> 1) * 1024 + (tl[g] & 1) * 32; }
  auto rdx = [&](int st, int k) { return *(const LAS bf16x8*)(xa[st % 3 == 2][k] + (st % 3 == 1 ? STG : 0)); };
  auto rdw = [&](int st, int k, int g) {
    const int o = (st % 3) * STG + k * PART, hi = o >= 6 * PART;
    return trfrag<256>(wa[hi][g] + (o - hi * 6 * PART));
  };
  f32x16 acc[3] = {};
  f32x4 cs = {};
  bf16x8 csel;
  { const u32 o = (c16 == (q & 1) * 4) ? 0x3f803f80u : 0u; csel = __builtin_bit_cast(bf16x8, u32x4{o, o, o, o}); }
  pdma(4); pdma(5); pdma(6);
  __builtin_amdgcn_raw_buffer_store_b128(u32x4{xt0[0], ep, xt0[1], ep}, rws, xto, 0, SC1);
  __builtin_amdgcn_raw_buffer_store_b128(u32x4{xt1[0], ep, xt1[1], ep}, rws, xto + 8 * 32768, 0, SC1);
  SCHED(); VBAR(7); SCHED();
  STAMP(3);
  bf16x8 X = rdx(0, 0), W[3] = {rdw(0, 0, 0), rdw(0, 0, 1), rdw(0, 0, 2)};
  u32 o_st, o_cs, o_u;
  float cp0, cp1;
  LAS char* csw;
  const u32 ld = (u32)(size_t)sm + kh * 20480 + fh * 10240 + th * 1024;
  const int rot = WROT, wrap = 43 - rot;
  auto kso = [&](int i, int lo) { return (u32)(((i >= wrap - lo ? kh * 43 + rot - 43 : kh * 43 + rot) + lo + i) * 16384); };
  const u32 csb = OFF_CS + (u32)(((xcd >> 2) * 16 + grp) * 8 * 1536);
  u32x2 sg = {0, 0}, cg[4] = {}; u32x4 uv0 = {0, 0, 0, 0}, uv1 = {0, 0, 0, 0};
  auto ldcs = [&]() { ALDW64(sg, o_st); aldw64o<0>(cg[0], o_cs, ws); aldw64o<1536>(cg[1], o_cs, ws); aldw64o<3072>(cg[2], o_cs, ws); aldw64o<-1536>(cg[3], o_cs, ws); };
  u32 ctag;
  auto cstag = [&]() { return ((sg[1] ^ ep) | (cg[0][1] ^ ep) | (cg[1][1] ^ ep)) | ((cg[2][1] ^ ep) | (cg[3][1] ^ ep)); };
  auto csred0 = [&]() {
    const u32 a0 = cg[0][0], a1 = cg[1][0], a2 = cg[2][0], a3 = cg[3][0];
    return ((__builtin_bit_cast(float, a3) + __builtin_bit_cast(float, a0)) + __builtin_bit_cast(float, a1)) + __builtin_bit_cast(float, a2);
  };
  auto csred1 = [&](float p) {
    const auto sw = __builtin_amdgcn_permlane32_swap(__builtin_bit_cast(u32, p), __builtin_bit_cast(u32, p), false, false);
    const u32 s0 = sw[0], s1 = sw[1];
    return __builtin_bit_cast(float, s0) + __builtin_bit_cast(float, s1);
  };
  auto csred2 = [&](float v) { const float hi = rbf(v); *(LAS u32*)csw = pk2(hi, v - hi); };
  auto side = [&](auto sc, auto nc) {
    constexpr int s = decltype(sc)::value, n = decltype(nc)::value;
    if constexpr (s == 0 && n >= 6 && n < 9) { wa[1][n - 6] = wa[0][n - 6] + 6 * PART; PIN(wa[1][n - 6]); }
    if constexpr (s == 1 && n < 4) { xa[1][n] = xa[0][n] + 2 * STG; PIN(xa[1][n]); }
    if constexpr (s == 1 && n == 7) {
      const float v = cs[0];
      __builtin_amdgcn_raw_buffer_store_b64(u32x2{__builtin_bit_cast(u32, v), ep}, rws, lane < 32 ? (u32)(lane * 8) : OOB, csb + dg * 1536 + tl[0] * 256, SC1);
    }
    if constexpr (s == 3 && n == 6) { o_st = OFF_ST + ((panel * 4 + tq) * 32 + r32) * 8; o_cs = csb + (h * 4 + 1) * 1536 + (w < 5 ? w : 5) * 256 + r32 * 8; PIN(o_st); PIN(o_cs); }
    if constexpr (s == 5 && n == 6) { csw = sm + M_CS + (w < 5 ? w : 5) * 128 + r32 * 4; PIN(csw); }
    if constexpr (s == 4 && n == 6) { o_u = OFF_XT + (idx >> 1) * 32768 + (w * 32 + (lane >> 2)) * 128 + (lane & 2) * 32 + (idx & 1) * 32 + (lane & 1) * 16; PIN(o_u); }
    if constexpr (s == 6 && n == 2) aldw64o<0>(cg[0], o_cs, ws);
    if constexpr (s == 6 && n == 6) aldw64o<1536>(cg[1], o_cs, ws);
    if constexpr (s == 6 && n == 7) aldw64o<3072>(cg[2], o_cs, ws);
    if constexpr (s == 6 && n == 8) aldw64o<-1536>(cg[3], o_cs, ws);
    if constexpr (s == 6 && n == 9) ALDW64(sg, o_st);
    if constexpr (s == 7 && n == 5) { wait5(sg, cg); cp0 = csred0(); PIN(cp0); }
    if constexpr (s == 7 && n == 6) { cp1 = csred1(cp0); PIN(cp1); }
    if constexpr (s == 7 && n == 7) csred2(cp1);
    if constexpr (s == 7 && n == 8) { ctag = cstag(); PIN(ctag); }
    if constexpr (s == 7 && n == 9) {
      if (__builtin_expect(!wall(ctag == 0u), 0)) {
#pragma unroll 1
        for (int i = 0; i < SPIN_G && !wall(cstag() == 0u); i++) { __builtin_amdgcn_s_sleep(8); ldcs(); wait0(sg, cg); }
        csred2(csred1(csred0()));
      }
    }
  };
  auto stage = [&](auto sc) {
    constexpr int s = decltype(sc)::value, SP = ((s + 2) % 3) * STG;
    auto dma = [&](auto ic) {
      constexpr int i = decltype(ic)::value;
      if constexpr (s >= 6) dma16<SP + i * 2048>(ld, wdo, rd, kso((s - 6) * 10 + i, fh * 5));
      else if constexpr (i < 3) dma16<SP + i * 1024>(lw, wo, i ? rw2 : rw, sow(s + 2) + i * 64);
      else dma16<SP + (i - 3) * 1024>(lx, i == 3 ? xo : xo2, rx, sox(s + 2));
    };
    auto slot = [&](auto nc) {
      constexpr int n = decltype(nc)::value, k = n / 3, g = n % 3;
      acc[g] = MFMA32(W[g], X, acc[g]);
      if constexpr (s == 0 && g == 0) cs = MFMA16(csel, W[0], cs);
      if constexpr (k < 3) W[g] = rdw(s, k + 1, g);
      if constexpr (n == 0) dma(IC<0>{});
      if constexpr (n == 1) dma(IC<1>{});
      if constexpr (n == 3) dma(IC<2>{});
      if constexpr (n == 4) dma(IC<3>{});
      if constexpr (n == 5) dma(IC<4>{});
      SCHED();
      side(sc, nc);
      SCHED();
    };
    bf16x8 X1 = rdx(s, 1);
    slot(IC<0>{}); slot(IC<1>{}); slot(IC<2>{});
    X = X1; X1 = rdx(s, 2);
    slot(IC<3>{}); slot(IC<4>{}); slot(IC<5>{});
    X = X1; X1 = rdx(s, 3);
    slot(IC<6>{}); slot(IC<7>{}); slot(IC<8>{});
    X = X1;
    slot(IC<9>{});
    GST(s);
    if (s == 0) VBAR(7);
    else if (s == 1 || s == 2) VBAR(6);
    else if (s == 6) VBAR(10);
    else if (s == 7) FBAR();
    else VBAR(5);
    if (s < 7) { X1 = rdx(s + 1, 0); W[0] = rdw(s + 1, 0, 0); }
    SCHED();
#pragma unroll
    for (int g = 1; g < 3; g++) { acc[g] = MFMA32(W[g], X, acc[g]); if (s < 7) W[g] = rdw(s + 1, 0, g); SCHED(); }
    X = X1;
  };
  stage(IC<0>{}); stage(IC<1>{}); stage(IC<2>{}); stage(IC<3>{}); stage(IC<4>{}); stage(IC<5>{}); stage(IC<6>{}); stage(IC<7>{});
  PST(4); GST(8);
  __builtin_amdgcn_s_setprio(2);
  ALDW128(uv0, o_u, 0); ALDW128(uv1, o_u, 2048);
  u32 e0, e1, d0, en; u32x2 ea, ec;
  {
    const LAS char* kd = sm + M_KD + kh * KDS + r32 * (KDR * 2);
    const int nx = r32 == 31 ? -31 * KDR * 2 : KDR * 2;
    const int ro = (16 + 2 * th + h) * 2;
    e0 = *(const LAS u16*)(kd + ro); e1 = *(const LAS u16*)(kd + (r32 < 31 ? nx + ro : (ro == 38 ? KDR * 2 : nx + ro + 2)));
    ea = *(const LAS u32x2*)(kd + (th ? 24 : 16 * h)); ec = *(const LAS u32x2*)(kd + 8);
    d0 = *(const LAS u32*)(kd + (h ? 31 * KDR * 2 : -KDR * 2) + 12);
    en = *(const LAS u16*)(kd + nx + (th ? 24 + 4 * h + (r32 == 31 ? 4 : 0) : (r32 == 31 ? 24 : 16)));
  }
  {
    const u32 stt = sg[0];
    const u32 nm = h ? 0u : (((stt & 0xffffu) ^ 0x8000u) * 0x10001u);
    const bf16x8 mb = __builtin_bit_cast(bf16x8, u32x4{nm, 0u, 0u, 0u});
#pragma unroll
    for (int g = 0; g < 3; g++) acc[g] = MFMA32(__builtin_bit_cast(bf16x8, u32x4{*(const LAS u32*)(sm + M_CS + tl[g] * 128 + r32 * 4), 0u, 0u, 0u}), mb, acc[g]);
    const float sd = bfhi(stt), c1 = -1.44269504088896341f * __builtin_amdgcn_rcpf(sd), s2 = sd * sd;
    const u32 ho = (u32)(lane * 16) + (u32)(OFF_HF + ((panel * 4 + tq) * KS2 + grp * 6) * 1024);
#pragma unroll
    for (int g = 0; g < 3; g++) {
      u32x4 o;
#pragma unroll
      for (int r = 0; r < 4; r++) {
        const f32x2 gg = {acc[g][2 * r], acc[g][2 * r + 1]}, u = {acc[g][2 * r + 8], acc[g][2 * r + 9]};
        const f32x2 t = gg * f32x2{c1, c1};
        const f32x2 e = {__builtin_amdgcn_exp2f(t[0]), __builtin_amdgcn_exp2f(t[1])};
        const f32x2 dn = __builtin_elementwise_fma(e, f32x2{s2, s2}, f32x2{s2, s2});
        o[r] = pkc(gg * u * f32x2{__builtin_amdgcn_rcpf(dn[0]), __builtin_amdgcn_rcpf(dn[1])});
      }
      __builtin_amdgcn_raw_buffer_store_b128(o, rws, ho + (grp * 6 + tl[g] < KS2 ? (u32)(tl[g] * 1024) : OOB), 0, SC1);
    }
  }
  SCHED();
  __builtin_amdgcn_s_setprio(0);
  PST(5);
  asm volatile("s_waitcnt vmcnt(3)" : "+v"(uv0), "+v"(uv1) :: "memory");
  auto uok = [&]() { return wall((((uv0[1] ^ ep) | (uv0[3] ^ ep)) | ((uv1[1] ^ ep) | (uv1[3] ^ ep))) == 0u); };
  if (__builtin_expect(!uok(), 0)) {
#pragma unroll 1
    for (int i = 0; i < SPIN_G && !uok(); i++) {
      __builtin_amdgcn_s_sleep(8);
      ALDW128(uv0, o_u, 0); ALDW128(uv1, o_u, 2048);
      asm volatile("s_waitcnt vmcnt(0)" : "+v"(uv0), "+v"(uv1) :: "memory");
    }
  }
  {
    LAS char* ud = sm + CV0 + (lane & 1) * CV_D + (w * 32 + (lane >> 2)) * 16 + (lane & 2) * 4;
    *(LAS u32x2*)(ud + CV_UA + 512) = u32x2{uv0[0], uv0[2]}; *(LAS u32x2*)(ud + CV_UA + 768) = u32x2{uv1[0], uv1[2]};
    *(LAS u32*)(ud + CV_UB + 516) = uv0[0]; *(LAS u32*)(ud + CV_UB + 520) = uv0[2]; *(LAS u32*)(ud + CV_UB + 772) = uv1[0]; *(LAS u32*)(ud + CV_UB + 776) = uv1[2];
  }
  LAS char* cc = sm + CV0 + kh * CV_D;
  const int T = tb * 2 + th;
  const u32 f2p = lane == 0 ? OFF_F2 + ((panel * 4 + tq) * 32 + grp * 2 + fh) * 128 : OOB;
  auto aoff = [&](int i) { int k = i >= 30 ? i - 30 : i; return ((k / 10 + 2) % 3) * STG + (k % 10) * 2048; };
  if (w >= 4) {
    lds_post(sm, M_P + w * 4, 1);
    const u32 l2 = (u32)(size_t)sm + STG + kh * 20480 + th * 1024;
    dma16<0>(l2, wdo, rd, kso(20, 0)); dma16<2048>(l2, wdo, rd, kso(21, 0)); dma16<4096>(l2, wdo, rd, kso(22, 0)); dma16<6144>(l2, wdo, rd, kso(23, 0));
    WAITV(4);
    __builtin_amdgcn_raw_buffer_store_b64(tag, rws, f2p, 0, SC1);
    PST(6);
    dma16<8192>(l2, wdo, rd, kso(24, 0)); dma16<10240>(l2, wdo, rd, kso(25, 0)); dma16<12288>(l2, wdo, rd, kso(26, 0));
    dma16<14336>(l2, wdo, rd, kso(27, 0)); dma16<16384>(l2, wdo, rd, kso(28, 0)); dma16<18432>(l2, wdo, rd, kso(29, 0));
    u32x4 wpark[13];
    auto rdd = __builtin_amdgcn_make_buffer_rsrc((void*)wd, 0, F * D * 2, 0x00020000);
#pragma unroll
    for (int k = 0; k < 13; k++) wpark[k] = __builtin_amdgcn_raw_buffer_load_b128(rdd, wdo, kso(30 + k, 0), 0);
    PST(7);
    const LAS char* ar0 = sm + kh * 20480 + (4 * h + q4) * 128 + (((q & 1) * 32 + (lane & 3) * 8) ^ ((q4 >> 1) * 64));
    const LAS char* ard[2][2] = {{ar0, (const LAS char*)((size_t)ar0 ^ 64)}, {ar0 + 2 * STG, (const LAS char*)((size_t)ar0 ^ 64) + 2 * STG}};
    LAS char* pk[2] = {sm + kh * 20480 + th * 1024 + lane * 16, sm + 2 * STG + kh * 20480 + th * 1024 + lane * 16};
    u32 hvo = OFF_HF + (T * KS2 + kh * 43 + rot - 43) * 1024 + lane * 16;
    PIN(ard[0][0]); PIN(ard[0][1]); PIN(ard[1][0]); PIN(ard[1][1]); PIN(pk[0]); PIN(pk[1]); PIN(hvo);
    auto rda = [&](int i, int dt) { const int o = aoff(i); return o >= 2 * STG ? trfrag<1024>(ard[1][dt] + (o - 2 * STG)) : trfrag<1024>(ard[0][dt] + o); };
    lds_wait<4>(sm, M_P, 1); lds_wait<4>(sm, M_P + 16, 1);
    bf16x8 A[3][2] = {{rda(0, 0), rda(0, 1)}, {rda(1, 0), rda(1, 1)}, {}};
    const u32 f2o = OFF_F2 + (T * 32 + kh * 14 + c16) * 128;
    u32x2 f2 = {0, 0};
#pragma unroll 1
    for (int i = 0; i < SPIN_G; i++) {
      ALDW64(f2, f2o);
      asm volatile("s_waitcnt vmcnt(0)" : "+v"(f2) :: "memory");
      if (wall(TAGOK(f2))) break;
    }
    PST(8);
    constexpr int HP = 12;
    u32x4 Hf[HP];
    auto ldh = [&](int i) { return __builtin_amdgcn_raw_buffer_load_b128(rws, hvo + (u32)(i & 3) * 1024u, (u32)((i & ~3) * 1024) + (i >= wrap ? 0u : 43u * 1024u), SC1); };
#pragma unroll
    for (int i = 0; i < HP; i++) Hf[i] = ldh(i);
    f32x16 a0 = {}, a1 = {};
    u32 pg = 0;
#pragma unroll
    for (int i = 0; i < 43; i++) {
      if (i == 13 && __builtin_expect((u32)__builtin_amdgcn_readfirstlane(pg) < 2u, 0)) lds_wait<1>(sm, M_P + (w ^ 1) * 4, 2);
      if (i == 28 && __builtin_expect((u32)__builtin_amdgcn_readfirstlane(pg) < 3u, 0)) lds_wait<1>(sm, M_P + (w ^ 1) * 4, 3);
      if (i == 13 || i == 28) asm volatile("" ::: "memory");
      if (i + 2 < 43) { A[(i + 2) % 3][0] = rda(i + 2, 0); A[(i + 2) % 3][1] = rda(i + 2, 1); }
      a0 = MFMA32(A[i % 3][0], __builtin_bit_cast(bf16x8, Hf[i % HP]), a0);
      a1 = MFMA32(A[i % 3][1], __builtin_bit_cast(bf16x8, Hf[i % HP]), a1);
      if (i + HP < 43) Hf[i % HP] = ldh(i + HP);
      if (i == 10) lds_post(sm, M_P + w * 4, 2);
      if (i == 12 || i == 27) pg = *(volatile LAS u32*)(sm + M_P + (w ^ 1) * 4);
      if (i >= 13 && i < 24) { const int o = aoff(i - 13); *(LAS u32x4*)(o >= 2 * STG ? pk[1] + (o - 2 * STG) : pk[0] + o) = wpark[i - 13]; }
      if (i == 22 || i == 23) *(LAS u32x4*)(pk[0] + aoff(i - 11)) = wpark[i - 11];
      if (i == 24) lds_post(sm, M_P + w * 4, 3);
      SCHED();
    }
    PST(9);
    asm volatile("" : "+v"(a0), "+v"(a1));
    {
      f32x16 snd = kh ? a0 : a1;
      LAS char* rb = sm + STG + kh * 20480 + th * 4096 + lane * 16;
#pragma unroll
      for (int g = 0; g < 4; g++) *(LAS f32x4*)(rb + g * 1024) = f32x4{snd[4 * g], snd[4 * g + 1], snd[4 * g + 2], snd[4 * g + 3]};
    }
    f32x16 kp = kh ? a1 : a0;
    const LAS char* rb = sm + STG + (kh ^ 1) * 20480 + th * 4096 + lane * 16;
    const LAS char* rr = cc + CV_RA + th * 4096 + lane * 16;
    u16* op = out + (size_t)(T * 32 + r32) * D + dg * 64 + kh * 32 + h * 8;
    PIN(rb); PIN(rr); PIN(op);
    FBAR();
    PST(10);
    u32x2 o[4];
#pragma unroll
    for (int g = 0; g < 4; g++) {
      const f32x4 v = (*(const LAS f32x4*)(rb + g * 1024) + f32x4{kp[4 * g], kp[4 * g + 1], kp[4 * g + 2], kp[4 * g + 3]}) + *(const LAS f32x4*)(rr + g * 1024);
      o[g] = u32x2{pk2(v[0], v[1]), pk2(v[2], v[3])};
    }
#pragma unroll
    for (int g = 0; g < 4; g += 2) {
      auto s0 = __builtin_amdgcn_permlane32_swap(o[g][0], o[g + 1][0], false, false);
      auto s1 = __builtin_amdgcn_permlane32_swap(o[g][1], o[g + 1][1], false, false);
      *(u32x4*)(op + g * 8) = u32x4{s0[0], s1[0], s0[1], s1[1]};
    }
  } else {
    WAITV(0);
    __builtin_amdgcn_raw_buffer_store_b64(tag, rws, f2p, 0, SC1);
    {
      LAS char* ra = cc + CV_RA + (255 - 64 * th - lane) * 16;
      const u32 SW = 0x01000302u, HL = 0x03020504u, HH = 0x03020302u;
      const u32 q0 = PERM(e0, e0), q1 = PERM(e1, e1), q10 = PERM(e0, e1);
      *(LAS u32x4*)(ra - 2048) = u32x4{q1, (lane & 1) ? q10 : q0, q0, q0};
      *(LAS u32x4*)(ra - 2048 + (CV_RB - CV_RA)) = u32x4{q1, (lane & 1) ? q1 : q10, q0, q0};
      u32x4 va, vb;
      if (th) {
        const u32 a0 = ea[0], a1 = ea[1], d1 = h ? a1 : a0, p0 = PERM(d1, d1), p1 = __builtin_amdgcn_perm(d1, d1, HH);
        va = u32x4{__builtin_amdgcn_perm(en, d1, HL), p1, p0, p0}; vb = u32x4{PERM(en, en), p1, __builtin_amdgcn_perm(d1, d1, SW), p0};
      } else {
        d0 = lane == 0 ? 0u : d0;
        const u32 d1 = ea[0], d2 = ea[1], d3 = ec[0], d4 = ec[1], p0 = PERM(d1, d1), p2 = PERM(d2, d2);
        const u32 s1 = __builtin_amdgcn_perm(d1, d1, SW), s2 = __builtin_amdgcn_perm(d2, d2, SW), t10 = __builtin_amdgcn_perm(d1, d0, HL), t21 = __builtin_amdgcn_perm(d2, d1, HL);
        va = h ? u32x4{s2, p2, s1, lane == 32 ? t10 : p0} : u32x4{__builtin_amdgcn_perm(d4, d3, HL), __builtin_amdgcn_perm(d3, d2, HL), t21, t10};
        vb = h ? u32x4{__builtin_amdgcn_perm(en, d2, HL), p2, t21, p0} : u32x4{__builtin_amdgcn_perm(d4, d4, SW), __builtin_amdgcn_perm(d3, d3, SW), s2, s1};
      }
      *(LAS u32x4*)ra = va; *(LAS u32x4*)(ra + (CV_RB - CV_RA)) = vb;
      LAS char* z = cc + lane * 8;
      *(LAS u32*)(z + th * (CV_UB + 4)) = 0u; *(LAS u32*)(z + th * (CV_UB + 4) + 4) = 0u;
      if (lane < 8) *(LAS u32x2*)(z + CV_RA + 4096 + th * (CV_RB - CV_RA)) = u32x2{0, 0};
    }
    lds_post(sm, M_P + w * 4, 1);
    const LAS char* ub = cc + ((c16 & 1) ? CV_UA : CV_UB) + 2 * (256 + 32 * (c16 >> 1) + 8 * q) - th * 256 - 192;
    const LAS char* tbp = cc + ((c16 & 1) ? CV_RB : CV_RA) + 2 * (2044 - 4 * (c16 >> 1) + 8 * q) - th * 256 - 7 * 512 - 192;
    u32 yso = OFF_YT + (2 * idx + kh) * L * 2 + (256 * (4 * th + (c16 & 1)) + 8 * q + 32 * (c16 >> 1)) * 2;
    PIN(ub); PIN(tbp); PIN(yso);
    lds_wait<4>(sm, M_P, 1); lds_wait<4>(sm, M_P + 16, 1);
    PST(6);
    f32x4 ya[8] = {};
    conv_main(ub, tbp, ya, [&](int m) { if (m == 108) lds_post(sm, M_P + w * 4, 2); });
    PST(7);
    lds_wait<1>(sm, M_P + (w ^ 1) * 4, 2);
#pragma unroll
    for (int a = 0; a < 4; a++) {
      f32x4 lo = ya[a], hi = ya[4 + a];
      asm volatile("" : "+v"(lo), "+v"(hi));
      *(LAS f32x4*)(cc + CV_RA + (th ^ 1) * 4096 + a * 1024 + lane * 16) = th ? lo : hi;
      ya[a] = th ? hi : lo;
    }
    lds_post(sm, M_P + w * 4, 3);
    lds_wait<1>(sm, M_P + (w ^ 1) * 4, 3);
    u32 p[4][2];
#pragma unroll
    for (int a = 0; a < 4; a++) {
      const f32x4 yv = ya[a] + *(const LAS f32x4*)(cc + CV_RA + th * 4096 + a * 1024 + lane * 16);
#pragma unroll
      for (int e = 0; e < 2; e++) { const u32 pv = pk2(yv[2 * e], yv[2 * e + 1]); p[a][e] = pv < 0xfffffffeu ? pv : 0xfffffffeu; }
    }
#pragma unroll
    for (int a = 0; a < 4; a += 2) {
      const bool odd = c16 & 1;
      u32 s0 = odd ? p[a][0] : p[a + 1][0], s1 = odd ? p[a][1] : p[a + 1][1];
      u32 r0 = __builtin_amdgcn_mov_dpp(s0, 0xB1, 0xf, 0xf, true), r1 = __builtin_amdgcn_mov_dpp(s1, 0xB1, 0xf, 0xf, true);
      u32x4 o = {odd ? r0 : p[a][0], odd ? p[a + 1][0] : r0, odd ? r1 : p[a][1], odd ? p[a + 1][1] : r1};
      __builtin_amdgcn_raw_buffer_store_b128(o, rws, yso + a * 512, 0, SC1);
    }
    PST(8);
    if (b == 0 && tid == 0) __builtin_amdgcn_raw_buffer_store_b32(ep, rws, 0, OFF_EP, SC1);
    const u16* xs = x + (size_t)(T * 32 + (lane >> 2)) * D + dg * 64 + kh * 32 + (lane & 3) * 8;
    const u32x4 x0 = *(const u32x4*)xs, x1 = *(const u32x4*)(xs + 16 * D);
    *(LAS u32x4*)(cc + 4096 + th * 2560 + (lane >> 2) * 80 + (lane & 3) * 16) = x0;
    *(LAS u32x4*)(cc + 4096 + th * 2560 + (16 + (lane >> 2)) * 80 + (lane & 3) * 16) = x1;
    lds_wait<1>(sm, M_P + 16 + w * 4, 2);
    const u32 yo = OFF_YT + ((dg * 64 + kh * 32 + (lane >> 2)) * L + T * 32 + (lane & 3) * 8) * 2;
    u32x4 y0, y1;
#pragma unroll 1
    for (int i = 0; i < SPIN_G; i++) {
      y0 = __builtin_amdgcn_raw_buffer_load_b128(rws, yo, 0, SC1); y1 = __builtin_amdgcn_raw_buffer_load_b128(rws, yo, 16 * L * 2, SC1);
      const u32x4 m = __builtin_elementwise_max(y0, y1);
      const u32 m0 = m[0] > m[1] ? m[0] : m[1], m1 = m[2] > m[3] ? m[2] : m[3];
      if (wall((m0 > m1 ? m0 : m1) != ~0u)) break;
      __builtin_amdgcn_s_sleep(4);
      asm volatile("" ::: "memory");
    }
    *(LAS u32x4*)(cc + th * 2048 + lane * 16) = y0;
    *(LAS u32x4*)(cc + th * 2048 + 1024 + lane * 16) = y1;
    const LAS char* yb = cc + th * 2048 + (4 * h + q4) * 64 + (q & 1) * 32 + (lane & 3) * 8;
    const LAS char* xb = cc + 4096 + th * 2560 + r32 * 80 + h * 8;
#pragma unroll
    for (int g = 0; g < 4; g++) {
      const u32x2 y = trread(yb + g * 512), xr = *(const LAS u32x2*)(xb + g * 16);
      const u32 ya0 = y[0], ya1 = y[1], xa0 = xr[0], xa1 = xr[1];
      *(LAS f32x4*)(cc + CV_RA + th * 4096 + g * 1024 + lane * 16) = f32x4{bflo(xa0) + bflo(ya0), bfhi(xa0) + bfhi(ya0), bflo(xa1) + bflo(ya1), bfhi(xa1) + bfhi(ya1)};
    }
    PST(9);
    FBAR();
  }
}

extern "C" void kernel_launch(void* const* d_in, const int* in_sizes, int n_in, void* d_out, int out_size, void* d_ws, size_t ws_size, hipStream_t stream) {
  const u16* const* in = (const u16* const*)d_in;
  k_fused<<<256, 512, 0, stream>>>(in[0], in[1], in[2], in[3], in[4], in[5], in[6], in[7], in[8], in[10], in[11], in[12], (char*)d_ws, (u16*)d_out);
}
```

```cpp
#include <hip/hip_runtime.h>
#include <hip/hip_bf16.h>
#include <stdint.h>

typedef unsigned short u16;
typedef unsigned int u32;
typedef __attribute__((ext_vector_type(8))) short bf16x8;
typedef __attribute__((ext_vector_type(4))) short s16x4;
typedef __attribute__((ext_vector_type(4))) float f32x4;
typedef __attribute__((ext_vector_type(16))) float f32x16;
typedef __attribute__((ext_vector_type(4))) u32 u32x4;
typedef __attribute__((ext_vector_type(2))) u32 u32x2;
typedef __attribute__((ext_vector_type(2))) float f32x2;
typedef __attribute__((ext_vector_type(2))) __bf16 bf2_t;
#define LAS __attribute__((address_space(3)))
#define DEVI __device__ __forceinline__

constexpr int L = 2048, D = 512, NS = 128, F = 1376;
constexpr int KS2 = 86;
constexpr u32 OFF_XT = 0;
constexpr u32 OFF_YT = 4u << 20;
constexpr u32 OFF_HF = 6u << 20;
constexpr u32 OFF_ST = 12u << 20;
constexpr u32 OFF_CS = OFF_ST + 65536;
constexpr u32 OFF_EP = OFF_CS + 32 * 8 * 192 * 8;
constexpr u32 OFF_F2 = OFF_EP + 4096;
constexpr u32 WS_USED = 13u << 20;

DEVI float bflo(u32 v) { return __builtin_bit_cast(float, v << 16); }
DEVI float bfhi(u32 v) { return __builtin_bit_cast(float, v & 0xffff0000u); }
DEVI u32 pk2(float lo, float hi) { f32x2 f = {lo, hi}; bf2_t b = __builtin_convertvector(f, bf2_t); return __builtin_bit_cast(u32, b); }
DEVI float rbf(float f) { return bflo(pk2(f, f)); }
DEVI bool wall(bool ok) { return __builtin_amdgcn_ballot_w64(!ok) == 0ull; }
#define WAITV(n) asm volatile("s_waitcnt vmcnt(" #n ")" ::: "memory")
#define FBAR() asm volatile("s_waitcnt lgkmcnt(0)\n\ts_barrier" ::: "memory")
#define VBAR(n) asm volatile("s_waitcnt vmcnt(" #n ") lgkmcnt(0)\n\ts_barrier" ::: "memory")
#define SCHED() __builtin_amdgcn_sched_barrier(0)
#define PIN(v) asm volatile("" : "+v"(v))
#define MFMA32(a, b, c) __builtin_amdgcn_mfma_f32_32x32x16_bf16(a, b, c, 0, 0, 0)
#define MFMA16(a, b, c) __builtin_amdgcn_mfma_f32_16x16x32_bf16(a, b, c, 0, 0, 0)
#define PERM(hi, lo) __builtin_amdgcn_perm(hi, lo, 0x05040100u)
#define SC1 16
#define OOB 0x80000000u
#define WROT ((tb & 3) * 11)
#define ROT dg
#define GST(s)
#define PST(i) STAMP(i)
template <int N> struct IC { static constexpr int value = N; };

#define DIAG_DECL
#define STAMP(i)

typedef f32x2 cf;
DEVI cf cmul(cf b, cf a) { const cf t = cf{a[0], a[0]} * b; return __builtin_elementwise_fma(cf{-a[1], a[1]}, cf{b[1], b[0]}, t); }
DEVI u32 pkc(cf v) { return __builtin_bit_cast(u32, __builtin_convertvector(v, bf2_t)); }
DEVI cf cexp2_(float lr, float rev, float x) {
  float e = __builtin_amdgcn_exp2f(lr * x), f = __builtin_amdgcn_fractf(rev * x);
  return cf{e, e} * cf{__builtin_amdgcn_cosf(f), __builtin_amdgcn_sinf(f)};
}
DEVI u32x4 mkrsrc(const void* p, u32 bytes) { u32x4 r = {(u32)(size_t)p, (u32)((size_t)p >> 32) & 0xffffu, bytes, 0x00020000u}; return r; }
template <int IMM>
DEVI void dma16(u32 lds, u32 voff, u32x4 rsrc, u32 soff) {
  asm volatile("s_add_u32 m0, %0, %4\n\ts_nop 0\n\tbuffer_load_dwordx4 %1, %2, %3 offen lds" :: "s"(lds), "v"(voff), "s"(rsrc), "s"(soff), "i"(IMM) : "scc");
}
DEVI u32x2 trread(const LAS char* p) { return __builtin_bit_cast(u32x2, __builtin_amdgcn_ds_read_tr16_b64_v4i16((LAS s16x4*)p)); }
template <int STEP>
DEVI bf16x8 trfrag(const LAS char* p) {
  s16x4 lo = __builtin_amdgcn_ds_read_tr16_b64_v4i16((LAS s16x4*)p), hi = __builtin_amdgcn_ds_read_tr16_b64_v4i16((LAS s16x4*)(p + STEP));
  return __builtin_shufflevector(lo, hi, 0, 1, 2, 3, 4, 5, 6, 7);
}
template <int CTRL, int RM> DEVI float dppadd(float v) { return v + __builtin_bit_cast(float, __builtin_amdgcn_update_dpp(0, __builtin_bit_cast(int, v), CTRL, RM, 0xf, true)); }
DEVI float lane63(float v) { return __builtin_bit_cast(float, __builtin_amdgcn_readlane(__builtin_bit_cast(int, v), 63)); }
DEVI float dot2(u32 a, u32 b, float c) { return __builtin_amdgcn_fdot2_f32_bf16(__builtin_bit_cast(bf2_t, a), __builtin_bit_cast(bf2_t, b), c, false); }

constexpr int PART = 10368, P_G = 4096, P_U = 7200;
constexpr int STG = 4 * PART;
constexpr int CV0 = 3 * STG;
constexpr int CV_UA = 0, CV_UB = 4640, CV_RA = 9344, CV_RB = 13568, CV_D = 17920;
constexpr int KX = 5888, KY = 8704, CV_LN = 2 * KX;
constexpr int MISC = CV0 + 2 * CV_D;
constexpr int M_P = MISC;
constexpr int M_KD = MISC + 48;
constexpr int KDR = 20, KDS = 32 * KDR * 2 + 16;
constexpr int M_CS = M_KD + 2 * KDS;
constexpr int LDS_BYTES = M_CS + 768;
constexpr u32 CODE_BYTES = 21 * 1024;
constexpr int SPIN_L = 1 << 18, SPIN_G = 1 << 16;

DEVI void lds_post(LAS char* sm, int word_off, u32 v) { if ((threadIdx.x & 63) == 0) *(volatile LAS u32*)(sm + word_off) = v; }
template <int N>
DEVI void lds_wait(LAS char* sm, int off, u32 v) {
#pragma unroll 1
  for (int i = 0; i < SPIN_L; i++) {
    u32 m;
    if (N == 1) m = *(volatile LAS u32*)(sm + off);
    else if (N == 2) { u32x2 p = *(volatile LAS u32x2*)(sm + off); m = p[0] < p[1] ? p[0] : p[1]; }
    else { u32x4 p = *(volatile LAS u32x4*)(sm + off); u32 a = p[0] < p[1] ? p[0] : p[1], b = p[2] < p[3] ? p[2] : p[3]; m = a < b ? a : b; }
    if ((u32)__builtin_amdgcn_readfirstlane(m) >= v) break;
    __builtin_amdgcn_s_sleep(1);
  }
  asm volatile("" ::: "memory");
}
#define ALD16(dst, base, voff) asm volatile("global_load_ushort %0, %1, %2" : "=v"(dst) : "v"(voff), "s"(base))
#define ALD128(dst, base, voff) asm volatile("global_load_dwordx4 %0, %1, %2" : "=v"(dst) : "v"(voff), "s"(base))
DEVI void aldw64(u32x2& dst, u32 voff, const char* base) { asm volatile("global_load_dwordx2 %0, %1, %2 sc1" : "+v"(dst) : "v"(voff), "s"(base)); }
template <int IMM> DEVI void aldw128(u32x4& dst, u32 voff, const char* base) { asm volatile("global_load_dwordx4 %0, %1, %2 offset:%3 sc1" : "+v"(dst) : "v"(voff), "s"(base), "i"(IMM)); }
template <int IMM> DEVI void aldw64o(u32x2& dst, u32 voff, const char* base) { asm volatile("global_load_dwordx2 %0, %1, %2 offset:%3 sc1" : "+v"(dst) : "v"(voff), "s"(base), "i"(IMM)); }
DEVI void wait0(u32x2& a, u32x2 (&c)[4]) { asm volatile("s_waitcnt vmcnt(0)" : "+v"(a), "+v"(c[0]), "+v"(c[1]), "+v"(c[2]), "+v"(c[3]) :: "memory"); }
DEVI void wait5(u32x2& a, u32x2 (&c)[4]) { asm volatile("s_waitcnt vmcnt(5)" : "+v"(a), "+v"(c[0]), "+v"(c[1]), "+v"(c[2]), "+v"(c[3]) :: "memory"); }
DEVI void aldpark(u32x4& dst, u32 voff, u32x4 rsrc, u32 soff) { asm volatile("buffer_load_dwordx4 %0, %1, %2, %3 offen" : "+v"(dst) : "v"(voff), "s"(rsrc), "s"(soff)); }
#define ALDW64(dst, voff) aldw64(dst, voff, ws)
#define ALDW128(dst, voff, imm) aldw128<imm>(dst, voff, ws)

template <class HOOK>
DEVI void conv_main(const LAS char* ub, const LAS char* tb, f32x4 (&acc)[8], HOOK&& hook) {
  auto rdU = [&](int r, int n) { return *(const LAS bf16x8*)(ub + (3 - r) * 64 + n * 512); };
  auto rdT = [&](int r, int n) {
    s16x4 lo = *(const volatile LAS s16x4*)(tb + (3 - r) * 64 + (7 - n) * 512), hi = *(const volatile LAS s16x4*)(tb + (3 - r) * 64 + (7 - n) * 512 + 8);
    return (bf16x8)__builtin_shufflevector(lo, hi, 0, 1, 2, 3, 4, 5, 6, 7);
  };
  bf16x8 U[2][8], T[2][8];
  U[0][0] = rdU(0, 0);
#pragma unroll
  for (int n = 0; n < 8; n++) T[0][n] = rdT(0, n);
#pragma unroll
  for (int n = 1; n < 8; n++) U[0][n] = rdU(0, n);
#pragma unroll
  for (int r = 0; r < 4; r++) {
    const int p = r & 1;
    int k = 0;
#pragma unroll
    for (int n = 0; n < 8; n++)
#pragma unroll
      for (int t = 0; t + n < 8; t++) {
        acc[t + n] = MFMA16(T[p][t], U[p][n], acc[t + n]);
        if (r < 3 && k < 32 && (k & 1)) { if (k < 16) T[p ^ 1][k >> 1] = rdT(r + 1, k >> 1); else U[p ^ 1][(k >> 1) - 8] = rdU(r + 1, (k >> 1) - 8); }
        hook(r * 36 + k);
        k++;
        SCHED();
      }
  }
}

__global__ __launch_bounds__(512, 2) void k_fused(const u16* __restrict__ x, const u16* __restrict__ Are, const u16* __restrict__ Aim,
    const u16* __restrict__ Bre, const u16* __restrict__ Bim, const u16* __restrict__ Cre, const u16* __restrict__ Cim,
    const u16* __restrict__ ldt, const u16* __restrict__ dsk, const u16* __restrict__ wg, const u16* __restrict__ wu,
    const u16* __restrict__ wd, char* ws, u16* __restrict__ out) {
  __shared__ __attribute__((aligned(16))) char smem[LDS_BYTES];
  LAS char* sm = (LAS char*)smem;
  const int b = blockIdx.x, tid = threadIdx.x, lane = tid & 63, w = __builtin_amdgcn_readfirstlane(tid >> 6);
  const int xcd = b & 7, j = b >> 3;
  DIAG_DECL STAMP(0);
  const int panel = (xcd >> 2) * 8 + (j & 7), grp = (xcd & 3) * 4 + (j >> 3);
  const int tb = 4 * xcd + (j >> 3), dg = j & 7;
  const int idx = 32 * xcd + j;
  const int tq = w & 3, fh = w >> 2;
  const int r32 = lane & 31, h = lane >> 5, c16 = lane & 15, q = lane >> 4, q4 = c16 >> 2;
  asm volatile("" :: "s"(wg), "s"(wu), "s"(wd), "s"(x), "s"(ws), "s"(dsk), "s"(out));
  u32 ep;
  asm volatile("s_load_dword %0, %1, 0x0" : "=s"(ep) : "s"(ws + OFF_EP));
  u32x4 xv; u32 p_ar, p_ai, p_br, p_bi, p_cr, p_ci, p_dt, p_ds;
  {
    const int dt_ = 2 * idx + (tq >> 1);
    const u32 g2 = (u32)(dt_ * NS + (w & 1) * 64 + lane) * 2u, d2 = (u32)dt_ * 2u;
    ALD16(p_ar, Are, g2); ALD16(p_ai, Aim, g2); ALD16(p_dt, ldt, d2);
    ALD128(xv, x, (u32)((idx * 8 + w) * 1024 + lane * 16));
    ALD16(p_br, Bre, g2); ALD16(p_bi, Bim, g2); ALD16(p_cr, Cre, g2); ALD16(p_ci, Cim, g2); ALD16(p_ds, dsk, (u32)(4 * idx + 2 * fh));
  }
  {
    const u32 co = tid < 12 ? (u32)((j & 15) * 12 + tid) * 128u : 0u;
    const char* pc = (const char*)__builtin_amdgcn_s_getpc();
    asm volatile("s_mov_b32 m0, %0\n\ts_nop 0\n\tglobal_load_lds_dword %1, %2" :: "s"((u32)(size_t)sm + M_CS), "v"(co < CODE_BYTES ? co : 0u), "s"(pc));
  }
  SCHED(); __builtin_amdgcn_s_barrier(); SCHED();
  auto rws = __builtin_amdgcn_make_buffer_rsrc(ws, 0, WS_USED, 0x00020000);
  __builtin_amdgcn_raw_buffer_store_b128(u32x4{~0u, ~0u, ~0u, ~0u}, rws, (u32)(OFF_YT + idx * 8192 + tid * 16), 0, SC1);
  SCHED();
  const u32x4 rw = mkrsrc(fh ? wu : wg, grp < 15 ? D * F * 2 : 0), rw2 = mkrsrc(fh ? wu : wg, grp < 14 ? D * F * 2 : 0);
  const u32 wo = (u32)((tq * 16 + (lane >> 2)) * (F * 2) + grp * 192 + (lane & 3) * 16);
  const u32 lx = (u32)(size_t)sm + tq * PART + fh * 2048, lw = (u32)(size_t)sm + tq * PART + (fh ? P_U : P_G);
  auto sow = [&](int st) { return (u32)(((st + ROT) & 7) * (64 * F * 2)); };
  dma16<0>(lw, wo, rw, sow(0)); dma16<1024>(lw, wo, rw2, sow(0) + 64); dma16<2048>(lw, wo, rw2, sow(0) + 128);
  SCHED();
  const u32x4 rx = mkrsrc(grp < 15 ? x : wd, grp < 15 ? L * D * 2 : F * D * 2);
  u32 xo = (u32)(((panel * 4 + tq) * 32 + fh * 16 + (lane >> 3)) * 1024 + (((lane & 7) ^ q) * 16)), xo2 = (xo ^ 64u) + 8192u;
  auto sox = [&](int st) { return (u32)(((st + ROT) & 7) * 128); };
  const int th = w & 1, kh = tq >> 1;
  u32 wdo = (u32)((th * 8 + (lane >> 3)) * 1024 + dg * 128 + (((lane & 7) ^ ((q & 1) << 2)) * 16));
  PIN(xo); PIN(xo2); PIN(wdo);
  auto pdma = [&](int i) {
    SCHED();
    if (i == 0) dma16<0>(lx, xo, rx, sox(0));
    if (i == 1) dma16<1024>(lx, xo2, rx, sox(0));
    if (i == 2) dma16<STG>(lw, wo, rw, sow(1));
    if (i == 3) dma16<STG + 1024>(lw, wo, rw2, sow(1) + 64);
    if (i == 4) dma16<STG + 2048>(lw, wo, rw2, sow(1) + 128);
    if (i == 5) dma16<STG>(lx, xo, rx, sox(1));
    if (i == 6) dma16<STG + 1024>(lx, xo2, rx, sox(1));
    SCHED();
  };
  LAS char* cb = sm + CV0 + fh * CV_D;
  const LAS char* xa[2][4]; const LAS char* wa[2];
  xa[0][0] = sm + tq * PART + r32 * 128 + ((h ^ ((r32 >> 1) & 7)) * 16);
  wa[0] = sm + ((q & 1) ? P_U : P_G) + (8 * h + q4) * 64 + (lane & 3) * 8 + fh * (1024 + 32);
  const int fo = c16 * 272 + h * 16;
  const LAS char* fxa = cb + (tq >> 1) * 16 * 272 + (q & 1) * KX + fo;
  const LAS char* fya = sm + 2 * STG + fh * 2 * KY + (tq & 1) * 16 * 272 + (q & 1) * KY + fo;
  const LAS char* xta = sm + CV0 + (q & 1) * CV_D + CV_LN + q4 * 1040 + (w * 64 + h * 16 + (lane & 3) * 4) * 2;
  u32 xto = OFF_XT + (u32)((w * 16 + h * 4 + q4) * 32768 + idx * 128 + (q & 1) * 64 + (lane & 3) * 16);
#pragma unroll
  for (int k = 1; k < 4; k++) { xa[0][k] = (const LAS char*)((size_t)xa[0][0] ^ (size_t)(k * 32)); PIN(xa[0][k]); }
  const LAS char* wb[2] = {wa[0] + (fh ? 1024 - 32 : 32), nullptr};
  const int kc = c16 + 16 * (tq & 1), kr = (tq >> 1) * 16 + 4 * q;
  int kwo = kc * (KDR * 2) + kr * 2, kwx = M_CS + 256 + lane * 8;
  PIN(kwo); PIN(kwx);
  kwo = M_KD + fh * KDS + (kr == 20 ? kwo + 31 * KDR * 2 : kwo);
  LAS char* kw = sm + (kr * 32 + kc <= 640 ? kwo : kwx);
  LAS char* lnw = cb + CV_LN + tq * 1040 + lane * 16;
  LAS char* tw = sm + 2 * STG + fh * STG + kh * (2 * KY + fh * (CV_D - 2 * KY)) + th * (KY + fh * (KX - KY)) + lane * 4;
  u32 sto = lane == 63 ? OFF_ST + (idx * 8 + w) * 8 : OOB, xto2 = xto + 8 * 32768, cso = (c16 == (q & 1) * 4) ? 0x3f803f80u : 0u, dsm = (tq == 0 && lane == 0) ? ~0u : 0u;
  if (lane == 0) *(LAS u32*)(sm + M_P + w * 4) = 0u;
  PIN(xa[0][0]); PIN(wa[0]); PIN(wb[0]); PIN(fxa); PIN(fya); PIN(xta); PIN(xto); PIN(xto2); PIN(kw); PIN(lnw); PIN(tw); PIN(sto); PIN(cso); PIN(dsm);
  asm volatile("s_waitcnt vmcnt(5) lgkmcnt(0)" : "+s"(ep), "+v"(xv), "+v"(p_ar), "+v"(p_ai), "+v"(p_dt), "+v"(p_br), "+v"(p_bi), "+v"(p_cr), "+v"(p_ci), "+v"(p_ds));
  ep += 1u;
  STAMP(1);
#define tag (u32x2{ep, ep ^ 0x5bd1e995u})
#define TAGOK(v) ((bool)(((v)[0] == ep) & ((v)[1] == (ep ^ 0x5bd1e995u))))
  {
    float s = 0.f, qq = 0.f;
#pragma unroll
    for (int k = 0; k < 4; k++) { const u32 v = xv[k]; s = dot2(v, 0x3f803f80u, s); qq = dot2(v, v, qq); }
    asm("s_nop 3\n\t"
        "v_add_f32_dpp %0, %0, %0 row_shr:1 row_mask:0xf bank_mask:0xf bound_ctrl:1\n\tv_add_f32_dpp %1, %1, %1 row_shr:1 row_mask:0xf bank_mask:0xf bound_ctrl:1\n\ts_nop 0\n\t"
        "v_add_f32_dpp %0, %0, %0 row_shr:2 row_mask:0xf bank_mask:0xf bound_ctrl:1\n\tv_add_f32_dpp %1, %1, %1 row_shr:2 row_mask:0xf bank_mask:0xf bound_ctrl:1\n\ts_nop 0\n\t"
        "v_add_f32_dpp %0, %0, %0 row_shr:4 row_mask:0xf bank_mask:0xf bound_ctrl:1\n\tv_add_f32_dpp %1, %1, %1 row_shr:4 row_mask:0xf bank_mask:0xf bound_ctrl:1\n\ts_nop 0\n\t"
        "v_add_f32_dpp %0, %0, %0 row_shr:8 row_mask:0xf bank_mask:0xf bound_ctrl:1\n\tv_add_f32_dpp %1, %1, %1 row_shr:8 row_mask:0xf bank_mask:0xf bound_ctrl:1\n\ts_nop 0\n\t"
        "v_add_f32_dpp %0, %0, %0 row_bcast:15 row_mask:0xa bank_mask:0xf\n\tv_add_f32_dpp %1, %1, %1 row_bcast:15 row_mask:0xa bank_mask:0xf\n\ts_nop 0\n\t"
        "v_add_f32_dpp %0, %0, %0 row_bcast:31 row_mask:0xc bank_mask:0xf\n\tv_add_f32_dpp %1, %1, %1 row_bcast:31 row_mask:0xc bank_mask:0xf"
        : "+v"(s), "+v"(qq));
    const float dt = rbf(__builtin_amdgcn_exp2f(bflo(p_dt) * 1.44269504088896341f));
    const float lr = dt * bflo(p_ar) * 1.44269504088896341f, rev = dt * bflo(p_ai) * (fh ? -0.15915494309189535f : 0.15915494309189535f);
    const cf e0 = cexp2_(lr, rev, fh ? 1.f : 8.f);
    const float m = s * (1.f / D), var = __builtin_fmaxf(__builtin_fmaf(-m, m, qq * (1.f / D)), 0.f);
    const u32 mv = pk2(m, var);
    const u32 sdp = pk2(__builtin_amdgcn_sqrtf(rbf(bfhi(mv) + 1e-5f)), 0.f);
    const float rs_ = __builtin_amdgcn_rcpf(bflo(sdp));
    const float rs = lane63(rs_), nb = lane63(-bflo(mv) * rs_);
    u32x4 o;
#pragma unroll
    for (int k = 0; k < 4; k++) { const u32 v = xv[k]; o[k] = pk2(__builtin_fmaf(bflo(v), rs, nb), __builtin_fmaf(bfhi(v), rs, nb)); }
    *(LAS u32x4*)lnw = o;
    __builtin_amdgcn_raw_buffer_store_b64(u32x2{PERM(sdp, mv), ep}, rws, sto, 0, SC1);
    if (fh == 0) {
      LAS char* ty = tw;
      cf Y[16];
      Y[0] = cf{1.f, 0.f}; Y[1] = e0;
#pragma unroll
      for (int hb = 1; hb < 16; hb <<= 1) {
        if (hb > 1) Y[hb] = cmul(Y[hb >> 1], Y[hb >> 1]);
#pragma unroll
        for (int c = 1; c < hb; c++) Y[hb + c] = cmul(Y[c], Y[hb]);
      }
      const cf y16 = cmul(Y[8], Y[8]);
#pragma unroll
      for (int c = 0; c < 16; c++) {
        cf t = c ? cmul(Y[c], y16) : y16;
        *(LAS u32*)(ty + c * 272) = pkc(Y[c]);
        *(LAS u32*)(ty + (16 + c) * 272) = pkc(t);
        if (c == 3) pdma(0);
        if (c == 11) pdma(1);
      }
    } else {
      LAS char* tx = tw;
      auto put = [&](int r, cf t) { *(LAS u32*)(tx + r * 272) = pkc(t); };
      const cf Bc = {dt * bflo(p_br), -dt * bflo(p_bi)}, Cc = {bflo(p_cr), -bflo(p_ci)};
      cf X[21];
      X[0] = cmul(Cc, Bc);
      const cf e1 = e0, e2 = cmul(e1, e1), e4 = cmul(e2, e2), e256 = cexp2_(lr, rev, 256.f);
      X[1] = cmul(X[0], e1); X[2] = cmul(X[0], e2); X[3] = cmul(X[1], e2);
      X[4] = cmul(X[0], e4); X[5] = cmul(X[1], e4); X[6] = cmul(X[2], e4); X[7] = cmul(X[3], e4);
#pragma unroll
      for (int r = 0; r < 8; r++) put(r, X[r]);
      pdma(0);
      X[8] = cmul(X[0], e256); X[9] = cmul(X[2], e256); X[10] = cmul(X[4], e256); X[11] = cmul(X[6], e256);
      X[12] = cmul(X[8], e256); X[13] = cmul(X[10], e256); X[14] = cmul(X[12], e256); X[15] = cmul(X[13], e256);
#pragma unroll
      for (int r = 8; r < 16; r++) put(r, X[r]);
      pdma(1);
      X[16] = cmul(X[14], e256); X[17] = cmul(X[16], e256); X[18] = cmul(X[17], e256); X[19] = cmul(X[18], e256); X[20] = cmul(X[19], e256);
#pragma unroll
      for (int r = 16; r < 21; r++) put(r, X[r]);
    }
  }
  SCHED(); VBAR(6); SCHED();
  STAMP(2);
  const u32x4 rd = mkrsrc(wd, F * D * 2);
  const u32x2 xt0 = trread(xta), xt1 = trread(xta + 64);
  {
    bf16x8 fx[8], fy[8];
#pragma unroll
    for (int k = 0; k < 8; k++) { fx[k] = *(const LAS bf16x8*)(fxa + k * 32); fy[k] = *(const LAS bf16x8*)(fya + k * 32); }
    pdma(2); pdma(3); pdma(4); pdma(5); pdma(6);
    __builtin_amdgcn_raw_buffer_store_b128(u32x4{xt0[0], ep, xt0[1], ep}, rws, xto, 0, SC1);
    __builtin_amdgcn_raw_buffer_store_b128(u32x4{xt1[0], ep, xt1[1], ep}, rws, xto2, 0, SC1);
    SCHED();
    f32x4 ka = {bflo(p_ds & dsm), 0.f, 0.f, 0.f};
#pragma unroll
    for (int k = 0; k < 8; k++) ka = MFMA16(fx[k], fy[k], ka);
    *(LAS u32x2*)kw = u32x2{pk2(ka[0], ka[1]), pk2(ka[2], ka[3])};
  }
  auto rdx = [&](int st, int k) { return *(const LAS bf16x8*)(xa[st % 3 == 2][k] + (st % 3 == 1 ? STG : 0)); };
  auto rdw = [&](int st, int k, int g) {
    const int o = (st % 3) * STG + k * PART, hi = o >= 6 * PART;
    return trfrag<256>((g == 1 ? wb[hi] : wa[hi]) + (o - hi * 6 * PART) + (g == 2 ? 1024 : 0));
  };
  f32x16 acc[3] = {};
  f32x4 cs[3] = {};
  const bf16x8 csel = __builtin_bit_cast(bf16x8, u32x4{cso, cso, cso, cso});
  SCHED(); VBAR(7); SCHED();
  STAMP(3);
  bf16x8 X = rdx(0, 0), W[3] = {rdw(0, 0, 0), rdw(0, 0, 1), rdw(0, 0, 2)};
  u32 o_st, o_cs, o_u;
  float cp0, cp1;
  LAS char* csw;
  const u32 ld = (u32)(size_t)sm + kh * 20480 + fh * 10240 + th * 1024;
  const int rot = WROT, wrap = 43 - rot;
  auto kso = [&](int i, int lo) { return (u32)(((i >= wrap - lo ? kh * 43 + rot - 43 : kh * 43 + rot) + lo + i) * 16384); };
  const u32 csb = OFF_CS + (u32)(((xcd >> 2) * 16 + grp) * 8 * 1536);
  u32x2 sg = {0, 0}, cg[4] = {}; u32x4 uv0 = {0, 0, 0, 0}, uv1 = {0, 0, 0, 0};
  auto ldcs = [&]() { ALDW64(sg, o_st); aldw64o<0>(cg[0], o_cs, ws); aldw64o<1536>(cg[1], o_cs, ws); aldw64o<3072>(cg[2], o_cs, ws); aldw64o<-1536>(cg[3], o_cs, ws); };
  u32 ctag;
  auto cstag = [&]() { return ((sg[1] ^ ep) | (cg[0][1] ^ ep) | (cg[1][1] ^ ep)) | ((cg[2][1] ^ ep) | (cg[3][1] ^ ep)); };
  auto csred0 = [&]() {
    const u32 a0 = cg[0][0], a1 = cg[1][0], a2 = cg[2][0], a3 = cg[3][0];
    return ((__builtin_bit_cast(float, a3) + __builtin_bit_cast(float, a0)) + __builtin_bit_cast(float, a1)) + __builtin_bit_cast(float, a2);
  };
  auto csred1 = [&](float p) {
    const auto sw = __builtin_amdgcn_permlane32_swap(__builtin_bit_cast(u32, p), __builtin_bit_cast(u32, p), false, false);
    const u32 s0 = sw[0], s1 = sw[1];
    return __builtin_bit_cast(float, s0) + __builtin_bit_cast(float, s1);
  };
  auto csred2 = [&](float v) { const float hi = rbf(v); *(LAS u32*)csw = pk2(hi, v - hi); };
  auto side = [&](auto sc, auto nc) {
    constexpr int s = decltype(sc)::value, n = decltype(nc)::value;
    if constexpr (s == 0 && n == 6) { wa[1] = wa[0] + 6 * PART; wb[1] = wb[0] + 6 * PART; PIN(wa[1]); PIN(wb[1]); }
    if constexpr (s == 1 && n < 4) { xa[1][n] = xa[0][n] + 2 * STG; PIN(xa[1][n]); }
    if constexpr (s == 1 && n == 7) {
      const float c0 = cs[0][0], c1 = cs[1][0], c2 = cs[2][0], v = tq == 0 ? c0 : (tq == 1 ? c1 : c2);
      __builtin_amdgcn_raw_buffer_store_b64(u32x2{__builtin_bit_cast(u32, v), ep}, rws, lane < 32 ? (u32)(lane * 8) : OOB, csb + dg * 1536 + (fh * 3 + (tq < 2 ? tq : 2)) * 256, SC1);
    }
    if constexpr (s == 3 && n == 6) { o_st = OFF_ST + ((panel * 4 + tq) * 32 + r32) * 8; o_cs = csb + (h * 4 + 1) * 1536 + (w < 5 ? w : 5) * 256 + r32 * 8; PIN(o_st); PIN(o_cs); }
    if constexpr (s == 5 && n == 6) { csw = sm + M_CS + (w < 5 ? w : 5) * 128 + r32 * 4; PIN(csw); }
    if constexpr (s == 4 && n == 6) { o_u = OFF_XT + (idx >> 1) * 32768 + (w * 32 + (lane >> 2)) * 128 + (lane & 2) * 32 + (idx & 1) * 32 + (lane & 1) * 16; PIN(o_u); }
    if constexpr (s == 6 && n == 2) aldw64o<0>(cg[0], o_cs, ws);
    if constexpr (s == 6 && n == 6) aldw64o<1536>(cg[1], o_cs, ws);
    if constexpr (s == 6 && n == 7) aldw64o<3072>(cg[2], o_cs, ws);
    if constexpr (s == 6 && n == 8) aldw64o<-1536>(cg[3], o_cs, ws);
    if constexpr (s == 6 && n == 9) ALDW64(sg, o_st);
    if constexpr (s == 7 && n == 5) { wait5(sg, cg); cp0 = csred0(); PIN(cp0); }
    if constexpr (s == 7 && n == 6) { cp1 = csred1(cp0); PIN(cp1); }
    if constexpr (s == 7 && n == 7) csred2(cp1);
    if constexpr (s == 7 && n == 8) { ctag = cstag(); PIN(ctag); }
    if constexpr (s == 7 && n == 9) {
      if (__builtin_expect(!wall(ctag == 0u), 0)) {
#pragma unroll 1
        for (int i = 0; i < SPIN_G && !wall(cstag() == 0u); i++) { __builtin_amdgcn_s_sleep(8); ldcs(); wait0(sg, cg); }
        csred2(csred1(csred0()));
      }
    }
  };
  auto stage = [&](auto sc) {
    constexpr int s = decltype(sc)::value, SP = ((s + 2) % 3) * STG;
    auto dma = [&](auto ic) {
      constexpr int i = decltype(ic)::value;
      if constexpr (s >= 6) dma16<SP + i * 2048>(ld, wdo, rd, kso((s - 6) * 10 + i, fh * 5));
      else if constexpr (i < 3) dma16<SP + i * 1024>(lw, wo, i ? rw2 : rw, sow(s + 2) + i * 64);
      else dma16<SP + (i - 3) * 1024>(lx, i == 3 ? xo : xo2, rx, sox(s + 2));
    };
    auto slot = [&](auto nc) {
      constexpr int n = decltype(nc)::value, k = n / 3, g = n % 3;
      acc[g] = MFMA32(W[g], X, acc[g]);
      if constexpr (s == 0) cs[g] = MFMA16(csel, W[g], cs[g]);
      if constexpr (k < 3) W[g] = rdw(s, k + 1, g);
      if constexpr (n == 0) dma(IC<0>{});
      if constexpr (n == 1) dma(IC<1>{});
      if constexpr (n == 3) dma(IC<2>{});
      if constexpr (n == 4) dma(IC<3>{});
      if constexpr (n == 5) dma(IC<4>{});
      SCHED();
      side(sc, nc);
      SCHED();
    };
    bf16x8 X1 = rdx(s, 1);
    slot(IC<0>{}); slot(IC<1>{}); slot(IC<2>{});
    X = X1; X1 = rdx(s, 2);
    slot(IC<3>{}); slot(IC<4>{}); slot(IC<5>{});
    X = X1; X1 = rdx(s, 3);
    slot(IC<6>{}); slot(IC<7>{}); slot(IC<8>{});
    X = X1;
    slot(IC<9>{});
    GST(s);
    if (s == 0) VBAR(7);
    else if (s == 1 || s == 2) VBAR(6);
    else if (s == 6) VBAR(10);
    else if (s == 7) FBAR();
    else VBAR(5);
    if (s < 7) { X1 = rdx(s + 1, 0); W[0] = rdw(s + 1, 0, 0); }
    SCHED();
#pragma unroll
    for (int g = 1; g < 3; g++) { acc[g] = MFMA32(W[g], X, acc[g]); if (s == 0) cs[g] = MFMA16(csel, W[g], cs[g]); if (s < 7) W[g] = rdw(s + 1, 0, g); SCHED(); }
    X = X1;
  };
  stage(IC<0>{}); stage(IC<1>{}); stage(IC<2>{}); stage(IC<3>{}); stage(IC<4>{}); stage(IC<5>{}); stage(IC<6>{}); stage(IC<7>{});
  PST(4); GST(8);
  __builtin_amdgcn_s_setprio(2);
  ALDW128(uv0, o_u, 0); ALDW128(uv1, o_u, 2048);
  u32 e0, e1, d0, en; u32x2 ea, ec;
  {
    const LAS char* kd = sm + M_KD + kh * KDS + r32 * (KDR * 2);
    const int nx = r32 == 31 ? -31 * KDR * 2 : KDR * 2;
    const int ro = (16 + 2 * th + h) * 2;
    e0 = *(const LAS u16*)(kd + ro); e1 = *(const LAS u16*)(kd + (r32 < 31 ? nx + ro : (ro == 38 ? KDR * 2 : nx + ro + 2)));
    ea = *(const LAS u32x2*)(kd + (th ? 24 : 16 * h)); ec = *(const LAS u32x2*)(kd + 8);
    d0 = *(const LAS u32*)(kd + (h ? 31 * KDR * 2 : -KDR * 2) + 12);
    en = *(const LAS u16*)(kd + nx + (th ? 24 + 4 * h + (r32 == 31 ? 4 : 0) : (r32 == 31 ? 24 : 16)));
  }
  {
    const u32 stt = sg[0];
    const u32 nm = h ? 0u : (((stt & 0xffffu) ^ 0x8000u) * 0x10001u);
    const bf16x8 mb = __builtin_bit_cast(bf16x8, u32x4{nm, 0u, 0u, 0u});
#pragma unroll
    for (int g = 0; g < 3; g++) acc[g] = MFMA32(__builtin_bit_cast(bf16x8, u32x4{*(const LAS u32*)(sm + M_CS + (fh * 3 + g) * 128 + r32 * 4), 0u, 0u, 0u}), mb, acc[g]);
    const float sd = bfhi(stt), c1 = -1.44269504088896341f * __builtin_amdgcn_rcpf(sd), s2 = sd * sd;
    const u32 ho = (u32)(lane * 16) + (u32)(OFF_HF + ((panel * 4 + tq) * KS2 + grp * 6 + 3 * fh) * 1024);
#pragma unroll
    for (int g = 0; g < 3; g++) {
      u32x4 o;
#pragma unroll
      for (int r = 0; r < 4; r++) {
        const f32x2 gg = {acc[g][2 * r], acc[g][2 * r + 1]}, u = {acc[g][2 * r + 8], acc[g][2 * r + 9]};
        const f32x2 t = gg * f32x2{c1, c1};
        const f32x2 e = {__builtin_amdgcn_exp2f(t[0]), __builtin_amdgcn_exp2f(t[1])};
        const f32x2 dn = __builtin_elementwise_fma(e, f32x2{s2, s2}, f32x2{s2, s2});
        o[r] = pkc(gg * u * f32x2{__builtin_amdgcn_rcpf(dn[0]), __builtin_amdgcn_rcpf(dn[1])});
      }
      __builtin_amdgcn_raw_buffer_store_b128(o, rws, grp * 6 + 3 * fh + g < KS2 ? ho + g * 1024 : OOB, 0, SC1);
    }
  }
  SCHED();
  __builtin_amdgcn_s_setprio(0);
  PST(5);
  asm volatile("s_waitcnt vmcnt(3)" : "+v"(uv0), "+v"(uv1) :: "memory");
  auto uok = [&]() { return wall((((uv0[1] ^ ep) | (uv0[3] ^ ep)) | ((uv1[1] ^ ep) | (uv1[3] ^ ep))) == 0u); };
  if (__builtin_expect(!uok(), 0)) {
#pragma unroll 1
    for (int i = 0; i < SPIN_G && !uok(); i++) {
      __builtin_amdgcn_s_sleep(8);
      ALDW128(uv0, o_u, 0); ALDW128(uv1, o_u, 2048);
      asm volatile("s_waitcnt vmcnt(0)" : "+v"(uv0), "+v"(uv1) :: "memory");
    }
  }
  {
    LAS char* ud = sm + CV0 + (lane & 1) * CV_D + (w * 32 + (lane >> 2)) * 16 + (lane & 2) * 4;
    *(LAS u32x2*)(ud + CV_UA + 512) = u32x2{uv0[0], uv0[2]}; *(LAS u32x2*)(ud + CV_UA + 768) = u32x2{uv1[0], uv1[2]};
    *(LAS u32*)(ud + CV_UB + 516) = uv0[0]; *(LAS u32*)(ud + CV_UB + 520) = uv0[2]; *(LAS u32*)(ud + CV_UB + 772) = uv1[0]; *(LAS u32*)(ud + CV_UB + 776) = uv1[2];
  }
  LAS char* cc = sm + CV0 + kh * CV_D;
  const int T = tb * 2 + th;
  const u32 f2p = lane < 16 ? OFF_F2 + ((panel * 4 + tq) * 32 + grp * 2 + fh) * 128 + lane * 8 : OOB;
  auto aoff = [&](int i) { int k = i >= 30 ? i - 30 : i; return ((k / 10 + 2) % 3) * STG + (k % 10) * 2048; };
  if (w >= 4) {
    lds_post(sm, M_P + w * 4, 1);
    const u32 l2 = (u32)(size_t)sm + STG + kh * 20480 + th * 1024;
    dma16<0>(l2, wdo, rd, kso(20, 0)); dma16<2048>(l2, wdo, rd, kso(21, 0)); dma16<4096>(l2, wdo, rd, kso(22, 0)); dma16<6144>(l2, wdo, rd, kso(23, 0));
    WAITV(4);
    __builtin_amdgcn_raw_buffer_store_b64(tag, rws, f2p, 0, SC1);
    PST(6);
    dma16<8192>(l2, wdo, rd, kso(24, 0)); dma16<10240>(l2, wdo, rd, kso(25, 0)); dma16<12288>(l2, wdo, rd, kso(26, 0));
    dma16<14336>(l2, wdo, rd, kso(27, 0)); dma16<16384>(l2, wdo, rd, kso(28, 0)); dma16<18432>(l2, wdo, rd, kso(29, 0));
    u32x4 wpark[13];
    auto rdd = __builtin_amdgcn_make_buffer_rsrc((void*)wd, 0, F * D * 2, 0x00020000);
#pragma unroll
    for (int k = 0; k < 13; k++) wpark[k] = __builtin_amdgcn_raw_buffer_load_b128(rdd, wdo, kso(30 + k, 0), 0);
    PST(7);
    const LAS char* ar0 = sm + kh * 20480 + (4 * h + q4) * 128 + (((q & 1) * 32 + (lane & 3) * 8) ^ ((q4 >> 1) * 64));
    const LAS char* ard[2][2] = {{ar0, (const LAS char*)((size_t)ar0 ^ 64)}, {ar0 + 2 * STG, (const LAS char*)((size_t)ar0 ^ 64) + 2 * STG}};
    LAS char* pk[2] = {sm + kh * 20480 + th * 1024 + lane * 16, sm + 2 * STG + kh * 20480 + th * 1024 + lane * 16};
    u32 hvo = OFF_HF + (T * KS2 + kh * 43 + rot - 43) * 1024 + lane * 16;
    PIN(ard[0][0]); PIN(ard[0][1]); PIN(ard[1][0]); PIN(ard[1][1]); PIN(pk[0]); PIN(pk[1]); PIN(hvo);
    auto rda = [&](int i, int dt) { const int o = aoff(i); return o >= 2 * STG ? trfrag<1024>(ard[1][dt] + (o - 2 * STG)) : trfrag<1024>(ard[0][dt] + o); };
    lds_wait<4>(sm, M_P, 1); lds_wait<4>(sm, M_P + 16, 1);
    bf16x8 A[3][2] = {{rda(0, 0), rda(0, 1)}, {rda(1, 0), rda(1, 1)}, {}};
    const u32 f2o = OFF_F2 + (T * 32 + kh * 14 + c16) * 128;
    u32x2 f2 = {0, 0};
#pragma unroll 1
    for (int i = 0; i < SPIN_G; i++) {
      ALDW64(f2, f2o);
      asm volatile("s_waitcnt vmcnt(0)" : "+v"(f2) :: "memory");
      if (wall(TAGOK(f2))) break;
    }
    PST(8);
    constexpr int HP = 12;
    u32x4 Hf[HP];
    auto ldh = [&](int i) { return __builtin_amdgcn_raw_buffer_load_b128(rws, hvo + (u32)(i & 3) * 1024u, (u32)((i & ~3) * 1024) + (i >= wrap ? 0u : 43u * 1024u), SC1); };
#pragma unroll
    for (int i = 0; i < HP; i++) Hf[i] = ldh(i);
    f32x16 a0 = {}, a1 = {};
    u32 pg = 0;
#pragma unroll
    for (int i = 0; i < 43; i++) {
      if (i == 13 && __builtin_expect((u32)__builtin_amdgcn_readfirstlane(pg) < 2u, 0)) lds_wait<1>(sm, M_P + (w ^ 1) * 4, 2);
      if (i == 28 && __builtin_expect((u32)__builtin_amdgcn_readfirstlane(pg) < 3u, 0)) lds_wait<1>(sm, M_P + (w ^ 1) * 4, 3);
      if (i == 13 || i == 28) asm volatile("" ::: "memory");
      if (i + 2 < 43) { A[(i + 2) % 3][0] = rda(i + 2, 0); A[(i + 2) % 3][1] = rda(i + 2, 1); }
      a0 = MFMA32(A[i % 3][0], __builtin_bit_cast(bf16x8, Hf[i % HP]), a0);
      a1 = MFMA32(A[i % 3][1], __builtin_bit_cast(bf16x8, Hf[i % HP]), a1);
      if (i + HP < 43) Hf[i % HP] = ldh(i + HP);
      if (i == 10) lds_post(sm, M_P + w * 4, 2);
      if (i == 12 || i == 27) pg = *(volatile LAS u32*)(sm + M_P + (w ^ 1) * 4);
      if (i >= 13 && i < 24) { const int o = aoff(i - 13); *(LAS u32x4*)(o >= 2 * STG ? pk[1] + (o - 2 * STG) : pk[0] + o) = wpark[i - 13]; }
      if (i == 22 || i == 23) *(LAS u32x4*)(pk[0] + aoff(i - 11)) = wpark[i - 11];
      if (i == 24) lds_post(sm, M_P + w * 4, 3);
      SCHED();
    }
    PST(9);
    asm volatile("" : "+v"(a0), "+v"(a1));
    {
      f32x16 snd = kh ? a0 : a1;
      LAS char* rb = sm + STG + kh * 20480 + th * 4096 + lane * 16;
#pragma unroll
      for (int g = 0; g < 4; g++) *(LAS f32x4*)(rb + g * 1024) = f32x4{snd[4 * g], snd[4 * g + 1], snd[4 * g + 2], snd[4 * g + 3]};
    }
    f32x16 kp = kh ? a1 : a0;
    const LAS char* rb = sm + STG + (kh ^ 1) * 20480 + th * 4096 + lane * 16;
    const LAS char* rr = cc + CV_RA + th * 4096 + lane * 16;
    u16* op = out + (size_t)(T * 32 + r32) * D + dg * 64 + kh * 32 + h * 8;
    PIN(rb); PIN(rr); PIN(op);
    FBAR();
    PST(10);
    u32x2 o[4];
#pragma unroll
    for (int g = 0; g < 4; g++) {
      const f32x4 v = (*(const LAS f32x4*)(rb + g * 1024) + f32x4{kp[4 * g], kp[4 * g + 1], kp[4 * g + 2], kp[4 * g + 3]}) + *(const LAS f32x4*)(rr + g * 1024);
      o[g] = u32x2{pk2(v[0], v[1]), pk2(v[2], v[3])};
    }
#pragma unroll
    for (int g = 0; g < 4; g += 2) {
      auto s0 = __builtin_amdgcn_permlane32_swap(o[g][0], o[g + 1][0], false, false);
      auto s1 = __builtin_amdgcn_permlane32_swap(o[g][1], o[g + 1][1], false, false);
      *(u32x4*)(op + g * 8) = u32x4{s0[0], s1[0], s0[1], s1[1]};
    }
  } else {
    WAITV(0);
    __builtin_amdgcn_raw_buffer_store_b64(tag, rws, f2p, 0, SC1);
    {
      LAS char* ra = cc + CV_RA + (255 - 64 * th - lane) * 16;
      const u32 SW = 0x01000302u, HL = 0x03020504u, HH = 0x03020302u;
      const u32 q0 = PERM(e0, e0), q1 = PERM(e1, e1), q10 = PERM(e0, e1);
      *(LAS u32x4*)(ra - 2048) = u32x4{q1, (lane & 1) ? q10 : q0, q0, q0};
      *(LAS u32x4*)(ra - 2048 + (CV_RB - CV_RA)) = u32x4{q1, (lane & 1) ? q1 : q10, q0, q0};
      u32x4 va, vb;
      if (th) {
        const u32 a0 = ea[0], a1 = ea[1], d1 = h ? a1 : a0, p0 = PERM(d1, d1), p1 = __builtin_amdgcn_perm(d1, d1, HH);
        va = u32x4{__builtin_amdgcn_perm(en, d1, HL), p1, p0, p0}; vb = u32x4{PERM(en, en), p1, __builtin_amdgcn_perm(d1, d1, SW), p0};
      } else {
        d0 = lane == 0 ? 0u : d0;
        const u32 d1 = ea[0], d2 = ea[1], d3 = ec[0], d4 = ec[1], p0 = PERM(d1, d1), p2 = PERM(d2, d2);
        const u32 s1 = __builtin_amdgcn_perm(d1, d1, SW), s2 = __builtin_amdgcn_perm(d2, d2, SW), t10 = __builtin_amdgcn_perm(d1, d0, HL), t21 = __builtin_amdgcn_perm(d2, d1, HL);
        va = h ? u32x4{s2, p2, s1, lane == 32 ? t10 : p0} : u32x4{__builtin_amdgcn_perm(d4, d3, HL), __builtin_amdgcn_perm(d3, d2, HL), t21, t10};
        vb = h ? u32x4{__builtin_amdgcn_perm(en, d2, HL), p2, t21, p0} : u32x4{__builtin_amdgcn_perm(d4, d4, SW), __builtin_amdgcn_perm(d3, d3, SW), s2, s1};
      }
      *(LAS u32x4*)ra = va; *(LAS u32x4*)(ra + (CV_RB - CV_RA)) = vb;
      LAS char* z = cc + lane * 8;
      *(LAS u32*)(z + th * (CV_UB + 4)) = 0u; *(LAS u32*)(z + th * (CV_UB + 4) + 4) = 0u;
      if (lane < 8) *(LAS u32x2*)(z + CV_RA + 4096 + th * (CV_RB - CV_RA)) = u32x2{0, 0};
    }
    lds_post(sm, M_P + w * 4, 1);
    const LAS char* ub = cc + ((c16 & 1) ? CV_UA : CV_UB) + 2 * (256 + 32 * (c16 >> 1) + 8 * q) - th * 256 - 192;
    const LAS char* tbp = cc + ((c16 & 1) ? CV_RB : CV_RA) + 2 * (2044 - 4 * (c16 >> 1) + 8 * q) - th * 256 - 7 * 512 - 192;
    u32 yso = OFF_YT + (2 * idx + kh) * L * 2 + (256 * (4 * th + (c16 & 1)) + 8 * q + 32 * (c16 >> 1)) * 2;
    PIN(ub); PIN(tbp); PIN(yso);
    lds_wait<4>(sm, M_P, 1); lds_wait<4>(sm, M_P + 16, 1);
    PST(6);
    f32x4 ya[8] = {};
    conv_main(ub, tbp, ya, [&](int m) { if (m == 108) lds_post(sm, M_P + w * 4, 2); });
    PST(7);
    lds_wait<1>(sm, M_P + (w ^ 1) * 4, 2);
#pragma unroll
    for (int a = 0; a < 4; a++) {
      f32x4 lo = ya[a], hi = ya[4 + a];
      asm volatile("" : "+v"(lo), "+v"(hi));
      *(LAS f32x4*)(cc + CV_RA + (th ^ 1) * 4096 + a * 1024 + lane * 16) = th ? lo : hi;
      ya[a] = th ? hi : lo;
    }
    lds_post(sm, M_P + w * 4, 3);
    lds_wait<1>(sm, M_P + (w ^ 1) * 4, 3);
    u32 p[4][2];
#pragma unroll
    for (int a = 0; a < 4; a++) {
      const f32x4 yv = ya[a] + *(const LAS f32x4*)(cc + CV_RA + th * 4096 + a * 1024 + lane * 16);
#pragma unroll
      for (int e = 0; e < 2; e++) { const u32 pv = pk2(yv[2 * e], yv[2 * e + 1]); p[a][e] = pv < 0xfffffffeu ? pv : 0xfffffffeu; }
    }
#pragma unroll
    for (int a = 0; a < 4; a += 2) {
      const bool odd = c16 & 1;
      u32 s0 = odd ? p[a][0] : p[a + 1][0], s1 = odd ? p[a][1] : p[a + 1][1];
      u32 r0 = __builtin_amdgcn_mov_dpp(s0, 0xB1, 0xf, 0xf, true), r1 = __builtin_amdgcn_mov_dpp(s1, 0xB1, 0xf, 0xf, true);
      u32x4 o = {odd ? r0 : p[a][0], odd ? p[a + 1][0] : r0, odd ? r1 : p[a][1], odd ? p[a + 1][1] : r1};
      __builtin_amdgcn_raw_buffer_store_b128(o, rws, yso + a * 512, 0, SC1);
    }
    PST(8);
    if (b == 0 && tid == 0) __builtin_amdgcn_raw_buffer_store_b32(ep, rws, 0, OFF_EP, SC1);
    const u16* xs = x + (size_t)(T * 32 + (lane >> 2)) * D + dg * 64 + kh * 32 + (lane & 3) * 8;
    const u32x4 x0 = *(const u32x4*)xs, x1 = *(const u32x4*)(xs + 16 * D);
    *(LAS u32x4*)(cc + 4096 + th * 2560 + (lane >> 2) * 80 + (lane & 3) * 16) = x0;
    *(LAS u32x4*)(cc + 4096 + th * 2560 + (16 + (lane >> 2)) * 80 + (lane & 3) * 16) = x1;
    lds_wait<1>(sm, M_P + 16 + w * 4, 3);
    const u32 yo = OFF_YT + ((dg * 64 + kh * 32 + (lane >> 2)) * L + T * 32 + (lane & 3) * 8) * 2;
    u32x4 y0, y1;
#pragma unroll 1
    for (int i = 0; i < SPIN_G; i++) {
      y0 = __builtin_amdgcn_raw_buffer_load_b128(rws, yo, 0, SC1); y1 = __builtin_amdgcn_raw_buffer_load_b128(rws, yo, 16 * L * 2, SC1);
      const u32x4 m = __builtin_elementwise_max(y0, y1);
      const u32 m0 = m[0] > m[1] ? m[0] : m[1], m1 = m[2] > m[3] ? m[2] : m[3];
      if (wall((m0 > m1 ? m0 : m1) != ~0u)) break;
      __builtin_amdgcn_s_sleep(8);
      asm volatile("" ::: "memory");
    }
    *(LAS u32x4*)(cc + th * 2048 + lane * 16) = y0;
    *(LAS u32x4*)(cc + th * 2048 + 1024 + lane * 16) = y1;
    const LAS char* yb = cc + th * 2048 + (4 * h + q4) * 64 + (q & 1) * 32 + (lane & 3) * 8;
    const LAS char* xb = cc + 4096 + th * 2560 + r32 * 80 + h * 8;
#pragma unroll
    for (int g = 0; g < 4; g++) {
      const u32x2 y = trread(yb + g * 512), xr = *(const LAS u32x2*)(xb + g * 16);
      const u32 ya0 = y[0], ya1 = y[1], xa0 = xr[0], xa1 = xr[1];
      *(LAS f32x4*)(cc + CV_RA + th * 4096 + g * 1024 + lane * 16) = f32x4{bflo(xa0) + bflo(ya0), bfhi(xa0) + bfhi(ya0), bflo(xa1) + bflo(ya1), bfhi(xa1) + bfhi(ya1)};
    }
    PST(9);
    FBAR();
  }
}

extern "C" void kernel_launch(void* const* d_in, const int* in_sizes, int n_in, void* d_out, int out_size, void* d_ws, size_t ws_size, hipStream_t stream) {
  const u16* const* in = (const u16* const*)d_in;
  k_fused<<<256, 512, 0, stream>>>(in[0], in[1], in[2], in[3], in[4], in[5], in[6], in[7], in[8], in[10], in[11], in[12], (char*)d_ws, (u16*)d_out);
}
```

```cpp
#include <hip/hip_runtime.h>
#include <hip/hip_bf16.h>
#include <stdint.h>

typedef unsigned short u16;
typedef unsigned int u32;
typedef __attribute__((ext_vector_type(8))) short bf16x8;
typedef __attribute__((ext_vector_type(4))) short s16x4;
typedef __attribute__((ext_vector_type(4))) float f32x4;
typedef __attribute__((ext_vector_type(16))) float f32x16;
typedef __attribute__((ext_vector_type(4))) u32 u32x4;
typedef __attribute__((ext_vector_type(2))) u32 u32x2;
typedef __attribute__((ext_vector_type(2))) float f32x2;
typedef __attribute__((ext_vector_type(2))) __bf16 bf2_t;
#define LAS __attribute__((address_space(3)))
#define DEVI __device__ __forceinline__

constexpr int L = 2048, D = 512, NS = 128, F = 1376;
constexpr int KS2 = 86;
constexpr u32 OFF_XT = 0;
constexpr u32 OFF_YT = 4u << 20;
constexpr u32 OFF_HF = 6u << 20;
constexpr u32 OFF_ST = 12u << 20;
constexpr u32 OFF_CS = OFF_ST + 65536;
constexpr u32 OFF_EP = OFF_CS + 32 * 8 * 192 * 8;
constexpr u32 OFF_F1 = OFF_EP + 4096;
constexpr u32 OFF_F2 = OFF_EP + 8192;
constexpr u32 WS_USED = 13u << 20;

DEVI float bflo(u32 v) { return __builtin_bit_cast(float, v << 16); }
DEVI float bfhi(u32 v) { return __builtin_bit_cast(float, v & 0xffff0000u); }
DEVI u32 pk2(float lo, float hi) { f32x2 f = {lo, hi}; bf2_t b = __builtin_convertvector(f, bf2_t); return __builtin_bit_cast(u32, b); }
DEVI float rbf(float f) { return bflo(pk2(f, f)); }
DEVI bool wall(bool ok) { return __builtin_amdgcn_ballot_w64(!ok) == 0ull; }
#define WAITV(n) asm volatile("s_waitcnt vmcnt(" #n ")" ::: "memory")
#define FBAR() asm volatile("s_waitcnt lgkmcnt(0)\n\ts_barrier" ::: "memory")
#define VBAR(n) asm volatile("s_waitcnt vmcnt(" #n ") lgkmcnt(0)\n\ts_barrier" ::: "memory")
#define SCHED() __builtin_amdgcn_sched_barrier(0)
#define PIN(v) asm volatile("" : "+v"(v))
#define MFMA32(a, b, c) __builtin_amdgcn_mfma_f32_32x32x16_bf16(a, b, c, 0, 0, 0)
#define MFMA16(a, b, c) __builtin_amdgcn_mfma_f32_16x16x32_bf16(a, b, c, 0, 0, 0)
#define PERM(hi, lo) __builtin_amdgcn_perm(hi, lo, 0x05040100u)
#define SC1 16
#define OOB 0x80000000u
#define WROT ((tb & 3) * 11)
#define ROT dg
#define GST(s)
#define PST(i) STAMP(i)
#define YGATE 3
#define PDELAY 4
template <int N> struct IC { static constexpr int value = N; };

#define DIAG_DECL
#define STAMP(i)

typedef f32x2 cf;
DEVI cf cmul(cf b, cf a) { const cf t = cf{a[0], a[0]} * b; return __builtin_elementwise_fma(cf{-a[1], a[1]}, cf{b[1], b[0]}, t); }
DEVI u32 pkc(cf v) { return __builtin_bit_cast(u32, __builtin_convertvector(v, bf2_t)); }
DEVI cf cexp2_(float lr, float rev, float x) {
  float e = __builtin_amdgcn_exp2f(lr * x), f = __builtin_amdgcn_fractf(rev * x);
  return cf{e, e} * cf{__builtin_amdgcn_cosf(f), __builtin_amdgcn_sinf(f)};
}
DEVI u32x4 mkrsrc(const void* p, u32 bytes) { u32x4 r = {(u32)(size_t)p, (u32)((size_t)p >> 32) & 0xffffu, bytes, 0x00020000u}; return r; }
template <int IMM>
DEVI void dma16(u32 lds, u32 voff, u32x4 rsrc, u32 soff) {
  asm volatile("s_add_u32 m0, %0, %4\n\ts_nop 0\n\tbuffer_load_dwordx4 %1, %2, %3 offen lds" :: "s"(lds), "v"(voff), "s"(rsrc), "s"(soff), "i"(IMM) : "scc");
}
DEVI u32x2 trread(const LAS char* p) { return __builtin_bit_cast(u32x2, __builtin_amdgcn_ds_read_tr16_b64_v4i16((LAS s16x4*)p)); }
template <int STEP>
DEVI bf16x8 trfrag(const LAS char* p) {
  s16x4 lo = __builtin_amdgcn_ds_read_tr16_b64_v4i16((LAS s16x4*)p), hi = __builtin_amdgcn_ds_read_tr16_b64_v4i16((LAS s16x4*)(p + STEP));
  return __builtin_shufflevector(lo, hi, 0, 1, 2, 3, 4, 5, 6, 7);
}
template <int CTRL, int RM> DEVI float dppadd(float v) { return v + __builtin_bit_cast(float, __builtin_amdgcn_update_dpp(0, __builtin_bit_cast(int, v), CTRL, RM, 0xf, true)); }
DEVI float lane63(float v) { return __builtin_bit_cast(float, __builtin_amdgcn_readlane(__builtin_bit_cast(int, v), 63)); }
DEVI float dot2(u32 a, u32 b, float c) { return __builtin_amdgcn_fdot2_f32_bf16(__builtin_bit_cast(bf2_t, a), __builtin_bit_cast(bf2_t, b), c, false); }

constexpr int PART = 10368, P_G = 4096, P_U = 7200;
constexpr int STG = 4 * PART;
constexpr int CV0 = 3 * STG;
constexpr int CV_UA = 0, CV_UB = 4640, CV_RA = 9344, CV_RB = 13568, CV_D = 17920;
constexpr int KX = 5888, KY = 8704, CV_LN = 2 * KX;
constexpr int MISC = CV0 + 2 * CV_D;
constexpr int M_P = MISC;
constexpr int M_KD = MISC + 48;
constexpr int KDR = 20, KDS = 32 * KDR * 2 + 16;
constexpr int M_CS = M_KD + 2 * KDS;
constexpr int LDS_BYTES = M_CS + 768;
constexpr u32 CODE_BYTES = 21 * 1024;
constexpr int SPIN_L = 1 << 18, SPIN_G = 1 << 16;

DEVI void lds_post(LAS char* p, u32 v) { if ((threadIdx.x & 63) == 0) *(volatile LAS u32*)p = v; }
DEVI void lds_wait(const LAS char* p, u32 v) {
  if (__builtin_expect(!wall(*(const volatile LAS u32*)p >= v), 0)) {
#pragma unroll 1
    for (int i = 0; i < SPIN_L; i++) { if (wall(*(const volatile LAS u32*)p >= v)) break; __builtin_amdgcn_s_sleep(1); }
  }
  asm volatile("" ::: "memory");
}
#define ALD16(dst, base, voff) asm volatile("global_load_ushort %0, %1, %2" : "=v"(dst) : "v"(voff), "s"(base))
#define ALD128(dst, base, voff) asm volatile("global_load_dwordx4 %0, %1, %2" : "=v"(dst) : "v"(voff), "s"(base))
DEVI void aldw64(u32x2& dst, u32 voff, const char* base) { asm volatile("global_load_dwordx2 %0, %1, %2 sc1" : "+v"(dst) : "v"(voff), "s"(base)); }
template <int IMM> DEVI void aldw128(u32x4& dst, u32 voff, const char* base) { asm volatile("global_load_dwordx4 %0, %1, %2 offset:%3 sc1" : "+v"(dst) : "v"(voff), "s"(base), "i"(IMM)); }
template <int IMM> DEVI void aldw64o(u32x2& dst, u32 voff, const char* base) { asm volatile("global_load_dwordx2 %0, %1, %2 offset:%3 sc1" : "+v"(dst) : "v"(voff), "s"(base), "i"(IMM)); }
DEVI void wait0(u32x2& a, u32x2 (&c)[4]) { asm volatile("s_waitcnt vmcnt(0)" : "+v"(a), "+v"(c[0]), "+v"(c[1]), "+v"(c[2]), "+v"(c[3]) :: "memory"); }
DEVI void wait5(u32x2& a, u32x2 (&c)[4]) { asm volatile("s_waitcnt vmcnt(5)" : "+v"(a), "+v"(c[0]), "+v"(c[1]), "+v"(c[2]), "+v"(c[3]) :: "memory"); }
DEVI void aldpark(u32x4& dst, u32 voff, u32x4 rsrc, u32 soff) { asm volatile("buffer_load_dwordx4 %0, %1, %2, %3 offen" : "+v"(dst) : "v"(voff), "s"(rsrc), "s"(soff)); }
#define ALDW64(dst, voff) aldw64(dst, voff, ws)
#define ALDW128(dst, voff, imm) aldw128<imm>(dst, voff, ws)

template <class HOOK>
DEVI void conv_main(const LAS char* ub, const LAS char* tb, f32x4 (&acc)[8], HOOK&& hook) {
  auto rdU = [&](int r, int n) { return *(const LAS bf16x8*)(ub + (3 - r) * 64 + n * 512); };
  auto rdT = [&](int r, int n) {
    s16x4 lo = *(const volatile LAS s16x4*)(tb + (3 - r) * 64 + (7 - n) * 512), hi = *(const volatile LAS s16x4*)(tb + (3 - r) * 64 + (7 - n) * 512 + 8);
    return (bf16x8)__builtin_shufflevector(lo, hi, 0, 1, 2, 3, 4, 5, 6, 7);
  };
  bf16x8 U[2][8], T[2][8];
  U[0][0] = rdU(0, 0);
#pragma unroll
  for (int n = 0; n < 8; n++) T[0][n] = rdT(0, n);
#pragma unroll
  for (int n = 1; n < 8; n++) U[0][n] = rdU(0, n);
#pragma unroll
  for (int r = 0; r < 4; r++) {
    const int p = r & 1;
    int k = 0;
#pragma unroll
    for (int n = 0; n < 8; n++)
#pragma unroll
      for (int t = 0; t + n < 8; t++) {
        acc[t + n] = MFMA16(T[p][t], U[p][n], acc[t + n]);
        if (r < 3 && k < 32 && (k & 1)) { if (k < 16) T[p ^ 1][k >> 1] = rdT(r + 1, k >> 1); else U[p ^ 1][(k >> 1) - 8] = rdU(r + 1, (k >> 1) - 8); }
        hook(r * 36 + k);
        k++;
        SCHED();
      }
  }
}

__global__ __launch_bounds__(512, 2) void k_fused(const u16* __restrict__ x, const u16* __restrict__ Are, const u16* __restrict__ Aim,
    const u16* __restrict__ Bre, const u16* __restrict__ Bim, const u16* __restrict__ Cre, const u16* __restrict__ Cim,
    const u16* __restrict__ ldt, const u16* __restrict__ dsk, const u16* __restrict__ wg, const u16* __restrict__ wu,
    const u16* __restrict__ wd, char* ws, u16* __restrict__ out) {
  __shared__ __attribute__((aligned(16))) char smem[LDS_BYTES];
  LAS char* sm = (LAS char*)smem;
  const int b = blockIdx.x, tid = threadIdx.x, lane = tid & 63, w = __builtin_amdgcn_readfirstlane(tid >> 6);
  const int xcd = b & 7, j = b >> 3;
  DIAG_DECL STAMP(0);
  const int panel = (xcd >> 2) * 8 + (j & 7), grp = (xcd & 3) * 4 + (j >> 3);
  const int tb = 4 * xcd + (j >> 3), dg = j & 7;
  const int idx = 32 * xcd + j;
  const int tq = w & 3, fh = w >> 2;
  const int r32 = lane & 31, h = lane >> 5, c16 = lane & 15, q = lane >> 4, q4 = c16 >> 2;
  asm volatile("" :: "s"(wg), "s"(wu), "s"(wd), "s"(x), "s"(ws), "s"(dsk), "s"(out));
  u32 ep;
  asm volatile("s_load_dword %0, %1, 0x0" : "=s"(ep) : "s"(ws + OFF_EP));
  u32x4 xv; u32 p_ar, p_ai, p_br, p_bi, p_cr, p_ci, p_dt, p_ds;
  {
    const int dt_ = 2 * idx + (tq >> 1);
    const u32 g2 = (u32)(dt_ * NS + (w & 1) * 64 + lane) * 2u, d2 = (u32)dt_ * 2u;
    ALD16(p_ar, Are, g2); ALD16(p_ai, Aim, g2); ALD16(p_dt, ldt, d2);
    ALD128(xv, x, (u32)((idx * 8 + w) * 1024 + lane * 16));
    ALD16(p_br, Bre, g2); ALD16(p_bi, Bim, g2); ALD16(p_cr, Cre, g2); ALD16(p_ci, Cim, g2); ALD16(p_ds, dsk, (u32)(4 * idx + 2 * fh));
  }
  {
    const u32 co = tid < 12 ? (u32)((j & 15) * 12 + tid) * 128u : 0u;
    const char* pc = (const char*)__builtin_amdgcn_s_getpc();
    asm volatile("s_mov_b32 m0, %0\n\ts_nop 0\n\tglobal_load_lds_dword %1, %2" :: "s"((u32)(size_t)sm + M_CS), "v"(co < CODE_BYTES ? co : 0u), "s"(pc));
  }
  SCHED(); __builtin_amdgcn_s_barrier(); SCHED();
  auto rws = __builtin_amdgcn_make_buffer_rsrc(ws, 0, WS_USED, 0x00020000);
  __builtin_amdgcn_raw_buffer_store_b128(u32x4{~0u, ~0u, ~0u, ~0u}, rws, (u32)(OFF_YT + idx * 8192 + tid * 16), 0, SC1);
  SCHED();
  const u32x4 rw = mkrsrc(fh ? wu : wg, grp < 15 ? D * F * 2 : 0), rw2 = mkrsrc(fh ? wu : wg, grp < 14 ? D * F * 2 : 0);
  const u32 wo = (u32)((tq * 16 + (lane >> 2)) * (F * 2) + grp * 192 + (lane & 3) * 16);
  const u32 lx = (u32)(size_t)sm + tq * PART + fh * 2048, lw = (u32)(size_t)sm + tq * PART + (fh ? P_U : P_G);
  auto sow = [&](int st) { return (u32)(((st + ROT) & 7) * (64 * F * 2)); };
  dma16<0>(lw, wo, rw, sow(0)); dma16<1024>(lw, wo, rw2, sow(0) + 64); dma16<2048>(lw, wo, rw2, sow(0) + 128);
  SCHED();
  const u32x4 rx = mkrsrc(grp < 15 ? x : wd, grp < 15 ? L * D * 2 : F * D * 2);
  u32 xo = (u32)(((panel * 4 + tq) * 32 + fh * 16 + (lane >> 3)) * 1024 + (((lane & 7) ^ q) * 16)), xo2 = (xo ^ 64u) + 8192u;
  auto sox = [&](int st) { return (u32)(((st + ROT) & 7) * 128); };
  const int th = w & 1, kh = tq >> 1;
  u32 wdo = (u32)((th * 8 + (lane >> 3)) * 1024 + dg * 128 + (((lane & 7) ^ ((q & 1) << 2)) * 16));
  PIN(xo); PIN(xo2); PIN(wdo);
  auto pdma = [&](int i) {
    SCHED();
    if (i == 0) dma16<0>(lx, xo, rx, sox(0));
    if (i == 1) dma16<1024>(lx, xo2, rx, sox(0));
    if (i == 2) dma16<STG>(lw, wo, rw, sow(1));
    if (i == 3) dma16<STG + 1024>(lw, wo, rw2, sow(1) + 64);
    if (i == 4) dma16<STG + 2048>(lw, wo, rw2, sow(1) + 128);
    if (i == 5) dma16<STG>(lx, xo, rx, sox(1));
    if (i == 6) dma16<STG + 1024>(lx, xo2, rx, sox(1));
    SCHED();
  };
  LAS char* cb = sm + CV0 + fh * CV_D;
  const LAS char* xa[2][4]; const LAS char* wa[2];
  xa[0][0] = sm + tq * PART + r32 * 128 + ((h ^ ((r32 >> 1) & 7)) * 16);
  wa[0] = sm + ((q & 1) ? P_U : P_G) + (8 * h + q4) * 64 + (lane & 3) * 8 + fh * (1024 + 32);
  const int fo = c16 * 272 + h * 16;
  const LAS char* fxa = cb + (tq >> 1) * 16 * 272 + (q & 1) * KX + fo;
  const LAS char* fya = sm + 2 * STG + fh * 2 * KY + (tq & 1) * 16 * 272 + (q & 1) * KY + fo;
  const LAS char* xta = sm + CV0 + CV_LN + q4 * 1040 + (w * 64 + q * 16 + (lane & 3) * 4) * 2;
  u32 xto = OFF_XT + (u32)((w * 8 + (lane >> 3)) * 32768 + idx * 128 + (lane & 7) * 16);
#pragma unroll
  for (int k = 1; k < 4; k++) { xa[0][k] = (const LAS char*)((size_t)xa[0][0] ^ (size_t)(k * 32)); PIN(xa[0][k]); }
  const LAS char* wb[2] = {wa[0] + (fh ? 1024 - 32 : 32), nullptr};
  const int kc = c16 + 16 * (tq & 1), kr = (tq >> 1) * 16 + 4 * q;
  int kwo = kc * (KDR * 2) + kr * 2, kwx = M_CS + 256 + lane * 8;
  PIN(kwo); PIN(kwx);
  kwo = M_KD + fh * KDS + (kr == 20 ? kwo + 31 * KDR * 2 : kwo);
  LAS char* kw = sm + (kr * 32 + kc <= 640 ? kwo : kwx);
  LAS char* lnw = cb + CV_LN + tq * 1040 + lane * 16;
  LAS char* tw = sm + 2 * STG + fh * STG + kh * (2 * KY + fh * (CV_D - 2 * KY)) + th * (KY + fh * (KX - KY)) + lane * 4;
  u32 sto = lane == 63 ? OFF_ST + (idx * 8 + w) * 8 : OOB, f1o = tid == 0 ? OFF_F1 + idx * 8 : OOB, cso = (c16 == (q & 1) * 4) ? 0x3f803f80u : 0u, dsm = (tq == 0 && lane == 0) ? ~0u : 0u;
  if (lane == 0) *(LAS u32*)(sm + M_P + w * 4) = 0u;
  PIN(xa[0][0]); PIN(wa[0]); PIN(wb[0]); PIN(fxa); PIN(fya); PIN(xta); PIN(xto); PIN(f1o); PIN(kw); PIN(lnw); PIN(tw); PIN(sto); PIN(cso); PIN(dsm);
  LAS char* cc = sm + CV0 + kh * CV_D;
  const int T = tb * 2 + th;
  const int rot = WROT, wrap = 43 - rot;
  LAS char* pme; const LAS char* ppar; const LAS char* pall; LAS char* ud; const LAS char* csr;
  u32 f2p, ho, ra_[13];
#define LP(k) ((LAS char*)(size_t)ra_[k])
  asm volatile("s_waitcnt vmcnt(5) lgkmcnt(0)" : "+s"(ep), "+v"(xv), "+v"(p_ar), "+v"(p_ai), "+v"(p_dt), "+v"(p_br), "+v"(p_bi), "+v"(p_cr), "+v"(p_ci), "+v"(p_ds));
  ep += 1u;
  STAMP(1);
#define tag (u32x2{ep, ep ^ 0x5bd1e995u})
#define TAGOK(v) ((bool)(((v)[0] == ep) & ((v)[1] == (ep ^ 0x5bd1e995u))))
  {
    float s = 0.f, qq = 0.f;
#pragma unroll
    for (int k = 0; k < 4; k++) { const u32 v = xv[k]; s = dot2(v, 0x3f803f80u, s); qq = dot2(v, v, qq); }
    asm("s_nop 3\n\t"
        "v_add_f32_dpp %0, %0, %0 row_shr:1 row_mask:0xf bank_mask:0xf bound_ctrl:1\n\tv_add_f32_dpp %1, %1, %1 row_shr:1 row_mask:0xf bank_mask:0xf bound_ctrl:1\n\ts_nop 0\n\t"
        "v_add_f32_dpp %0, %0, %0 row_shr:2 row_mask:0xf bank_mask:0xf bound_ctrl:1\n\tv_add_f32_dpp %1, %1, %1 row_shr:2 row_mask:0xf bank_mask:0xf bound_ctrl:1\n\ts_nop 0\n\t"
        "v_add_f32_dpp %0, %0, %0 row_shr:4 row_mask:0xf bank_mask:0xf bound_ctrl:1\n\tv_add_f32_dpp %1, %1, %1 row_shr:4 row_mask:0xf bank_mask:0xf bound_ctrl:1\n\ts_nop 0\n\t"
        "v_add_f32_dpp %0, %0, %0 row_shr:8 row_mask:0xf bank_mask:0xf bound_ctrl:1\n\tv_add_f32_dpp %1, %1, %1 row_shr:8 row_mask:0xf bank_mask:0xf bound_ctrl:1\n\ts_nop 0\n\t"
        "v_add_f32_dpp %0, %0, %0 row_bcast:15 row_mask:0xa bank_mask:0xf\n\tv_add_f32_dpp %1, %1, %1 row_bcast:15 row_mask:0xa bank_mask:0xf\n\ts_nop 0\n\t"
        "v_add_f32_dpp %0, %0, %0 row_bcast:31 row_mask:0xc bank_mask:0xf\n\tv_add_f32_dpp %1, %1, %1 row_bcast:31 row_mask:0xc bank_mask:0xf"
        : "+v"(s), "+v"(qq));
    const float dt = rbf(__builtin_amdgcn_exp2f(bflo(p_dt) * 1.44269504088896341f));
    const float lr = dt * bflo(p_ar) * 1.44269504088896341f, rev = dt * bflo(p_ai) * (fh ? -0.15915494309189535f : 0.15915494309189535f);
    const cf e0 = cexp2_(lr, rev, fh ? 1.f : 8.f);
    const float m = s * (1.f / D), var = __builtin_fmaxf(__builtin_fmaf(-m, m, qq * (1.f / D)), 0.f);
    const u32 mv = pk2(m, var);
    const u32 sdp = pk2(__builtin_amdgcn_sqrtf(rbf(bfhi(mv) + 1e-5f)), 0.f);
    const float rs_ = __builtin_amdgcn_rcpf(bflo(sdp));
    const float rs = lane63(rs_), nb = lane63(-bflo(mv) * rs_);
    u32x4 o;
#pragma unroll
    for (int k = 0; k < 4; k++) { const u32 v = xv[k]; o[k] = pk2(__builtin_fmaf(bflo(v), rs, nb), __builtin_fmaf(bfhi(v), rs, nb)); }
    *(LAS u32x4*)lnw = o;
    __builtin_amdgcn_raw_buffer_store_b64(u32x2{PERM(sdp, mv), ep}, rws, sto, 0, SC1);
    if (fh == 0) {
      LAS char* ty = tw;
      cf Y[16];
      Y[0] = cf{1.f, 0.f}; Y[1] = e0;
#pragma unroll
      for (int hb = 1; hb < 16; hb <<= 1) {
        if (hb > 1) Y[hb] = cmul(Y[hb >> 1], Y[hb >> 1]);
#pragma unroll
        for (int c = 1; c < hb; c++) Y[hb + c] = cmul(Y[c], Y[hb]);
      }
      const cf y16 = cmul(Y[8], Y[8]);
#pragma unroll
      for (int c = 0; c < 16; c++) {
        cf t = c ? cmul(Y[c], y16) : y16;
        *(LAS u32*)(ty + c * 272) = pkc(Y[c]);
        *(LAS u32*)(ty + (16 + c) * 272) = pkc(t);
        if (c == 3) pdma(0);
        if (c == 11) pdma(1);
      }
      SCHED();
      const u32 kd = (u32)(size_t)sm + M_KD + kh * KDS + r32 * (KDR * 2);
      const int nx = r32 == 31 ? -31 * KDR * 2 : KDR * 2;
      const int ro = (16 + 2 * th + h) * 2;
      ra_[0] = kd; ra_[1] = kd + ro; ra_[2] = kd + (r32 < 31 ? nx + ro : (ro == 38 ? KDR * 2 : nx + ro + 2));
      ra_[3] = kd + (th ? 24 : 16 * h); ra_[4] = kd + (h ? 31 * KDR * 2 : -KDR * 2);
      ra_[5] = kd + nx + 16 + th * (8 + 4 * h) + (r32 == 31 ? 8 - 4 * th : 0);
      ra_[6] = (u32)(size_t)cc + (255 - 64 * th - lane) * 16;
      ra_[7] = (u32)(size_t)cc + lane * 8;
      ra_[8] = (u32)(size_t)cc + ((c16 & 1) ? CV_UA : CV_UB) + 2 * (256 + 32 * (c16 >> 1) + 8 * q) - th * 256 - 192;
      ra_[9] = (u32)(size_t)cc + ((c16 & 1) ? CV_RB : CV_RA) + 2 * (2044 - 4 * (c16 >> 1) + 8 * q) - th * 256 - 7 * 512 - 192;
      ra_[10] = OFF_YT + (2 * idx + kh) * L * 2 + (256 * (4 * th + (c16 & 1)) + 8 * q + 32 * (c16 >> 1)) * 2;
      ra_[11] = (u32)(((T * 32 + (lane >> 2)) * D + dg * 64 + kh * 32 + (lane & 3) * 8) * 2);
      ra_[12] = (u32)(size_t)cc + CV_RA + th * 4096 + lane * 16;
#pragma unroll
      for (int k = 0; k < 13; k++) PIN(ra_[k]);
    } else {
      LAS char* tx = tw;
      auto put = [&](int r, cf t) { *(LAS u32*)(tx + r * 272) = pkc(t); };
      const cf Bc = {dt * bflo(p_br), -dt * bflo(p_bi)}, Cc = {bflo(p_cr), -bflo(p_ci)};
      cf X[21];
      X[0] = cmul(Cc, Bc);
      const cf e1 = e0, e2 = cmul(e1, e1), e4 = cmul(e2, e2), e256 = cexp2_(lr, rev, 256.f);
      X[1] = cmul(X[0], e1); X[2] = cmul(X[0], e2); X[3] = cmul(X[1], e2);
      X[4] = cmul(X[0], e4); X[5] = cmul(X[1], e4); X[6] = cmul(X[2], e4); X[7] = cmul(X[3], e4);
#pragma unroll
      for (int r = 0; r < 8; r++) put(r, X[r]);
      pdma(0);
      X[8] = cmul(X[0], e256); X[9] = cmul(X[2], e256); X[10] = cmul(X[4], e256); X[11] = cmul(X[6], e256);
      X[12] = cmul(X[8], e256); X[13] = cmul(X[10], e256); X[14] = cmul(X[12], e256); X[15] = cmul(X[13], e256);
#pragma unroll
      for (int r = 8; r < 16; r++) put(r, X[r]);
      pdma(1);
      X[16] = cmul(X[14], e256); X[17] = cmul(X[16], e256); X[18] = cmul(X[17], e256); X[19] = cmul(X[18], e256); X[20] = cmul(X[19], e256);
#pragma unroll
      for (int r = 16; r < 21; r++) put(r, X[r]);
    }
  }
  SCHED(); FBAR(); SCHED();
  STAMP(2);
  const u32x4 rd = mkrsrc(wd, F * D * 2);
  const bf16x8 xt = trfrag<CV_D>(xta);
  {
    bf16x8 fx[8], fy[8];
#pragma unroll
    for (int k = 0; k < 8; k++) { fx[k] = *(const LAS bf16x8*)(fxa + k * 32); fy[k] = *(const LAS bf16x8*)(fya + k * 32); }
    pdma(2); pdma(3); pdma(4); pdma(5); pdma(6);
    __builtin_amdgcn_raw_buffer_store_b128(__builtin_bit_cast(u32x4, xt), rws, xto, 0, SC1);
    SCHED();
    f32x4 ka = {bflo(p_ds & dsm), 0.f, 0.f, 0.f};
#pragma unroll
    for (int k = 0; k < 8; k++) ka = MFMA16(fx[k], fy[k], ka);
    *(LAS u32x2*)kw = u32x2{pk2(ka[0], ka[1]), pk2(ka[2], ka[3])};
  }
  auto rdx = [&](int st, int k) { return *(const LAS bf16x8*)(xa[st % 3 == 2][k] + (st % 3 == 1 ? STG : 0)); };
  auto rdw = [&](int st, int k, int g) {
    const int o = (st % 3) * STG + k * PART, hi = o >= 6 * PART;
    return trfrag<256>((g == 1 ? wb[hi] : wa[hi]) + (o - hi * 6 * PART) + (g == 2 ? 1024 : 0));
  };
  f32x16 acc[3] = {};
  f32x4 cs[3] = {};
  const bf16x8 csel = __builtin_bit_cast(bf16x8, u32x4{cso, cso, cso, cso});
  SCHED(); VBAR(6); SCHED();
  STAMP(3);
  bf16x8 X = rdx(0, 0), W[3] = {rdw(0, 0, 0), rdw(0, 0, 1), rdw(0, 0, 2)};
  u32 o_st, o_cs, o_u, o_fx;
  u32x2 fx2 = {0, 0};
  bool early;
  float cp0, cp1;
  LAS char* csw;
  const u32 ld = (u32)(size_t)sm + kh * 20480 + fh * 10240 + th * 1024;
  auto kso = [&](int i, int lo) { return (u32)(((i >= wrap - lo ? kh * 43 + rot - 43 : kh * 43 + rot) + lo + i) * 16384); };
  const u32 csb = OFF_CS + (u32)(((xcd >> 2) * 16 + grp) * 8 * 1536);
  u32x2 sg = {0, 0}, cg[4] = {}; u32x4 uv0 = {0, 0, 0, 0};
  auto ldcs = [&]() { ALDW64(sg, o_st); aldw64o<0>(cg[0], o_cs, ws); aldw64o<1536>(cg[1], o_cs, ws); aldw64o<3072>(cg[2], o_cs, ws); aldw64o<-1536>(cg[3], o_cs, ws); };
  u32 ctag;
  auto cstag = [&]() { return ((sg[1] ^ ep) | (cg[0][1] ^ ep) | (cg[1][1] ^ ep)) | ((cg[2][1] ^ ep) | (cg[3][1] ^ ep)); };
  auto csred0 = [&]() {
    const u32 a0 = cg[0][0], a1 = cg[1][0], a2 = cg[2][0], a3 = cg[3][0];
    return ((__builtin_bit_cast(float, a3) + __builtin_bit_cast(float, a0)) + __builtin_bit_cast(float, a1)) + __builtin_bit_cast(float, a2);
  };
  auto csred1 = [&](float p) {
    const auto sw = __builtin_amdgcn_permlane32_swap(__builtin_bit_cast(u32, p), __builtin_bit_cast(u32, p), false, false);
    const u32 s0 = sw[0], s1 = sw[1];
    return __builtin_bit_cast(float, s0) + __builtin_bit_cast(float, s1);
  };
  auto csred2 = [&](float v) { const float hi = rbf(v); *(LAS u32*)csw = pk2(hi, v - hi); };
  auto side = [&](auto sc, auto nc) {
    constexpr int s = decltype(sc)::value, n = decltype(nc)::value;
    if constexpr (s == 0 && n == 6) { wa[1] = wa[0] + 6 * PART; wb[1] = wb[0] + 6 * PART; PIN(wa[1]); PIN(wb[1]); }
    if constexpr (s == 1 && n < 4) { xa[1][n] = xa[0][n] + 2 * STG; PIN(xa[1][n]); }
    if constexpr (s == 1 && n == 7) {
      const float c0 = cs[0][0], c1 = cs[1][0], c2 = cs[2][0], v = tq == 0 ? c0 : (tq == 1 ? c1 : c2);
      __builtin_amdgcn_raw_buffer_store_b64(u32x2{__builtin_bit_cast(u32, v), ep}, rws, lane < 32 ? (u32)(lane * 8) : OOB, csb + dg * 1536 + (fh * 3 + (tq < 2 ? tq : 2)) * 256, SC1);
    }
    if constexpr (s == 3 && n == 6) { o_st = OFF_ST + ((panel * 4 + tq) * 32 + r32) * 8; o_cs = csb + (h * 4 + 1) * 1536 + (w < 5 ? w : 5) * 256 + r32 * 8; PIN(o_st); PIN(o_cs); }
    if constexpr (s == 2 && n == 2) { pme = sm + M_P + w * 4; ppar = sm + M_P + (w ^ 1) * 4; pall = sm + M_P + (lane & 7) * 4; PIN(pme); PIN(ppar); PIN(pall); }
    if constexpr (s == 3 && n == 2) { ud = sm + CV0 + (lane & 1) * CV_D + (w * 32 + (lane >> 1)) * 16; PIN(ud); }
    if constexpr (s == 4 && n == 2) { f2p = lane < 16 ? OFF_F2 + ((panel * 4 + tq) * 32 + grp * 2 + fh) * 128 + lane * 8 : OOB; PIN(f2p); }
    if constexpr (s == 5 && n == 2) { ho = (u32)(lane * 16) + (u32)(OFF_HF + ((panel * 4 + tq) * KS2 + grp * 6 + 3 * fh) * 1024); csr = sm + M_CS + fh * 384 + r32 * 4; PIN(ho); PIN(csr); }
    if constexpr (s == 5 && n == 6) { csw = sm + M_CS + (w < 5 ? w : 5) * 128 + r32 * 4; PIN(csw); }
    if constexpr (s == 2 && n == 7) __builtin_amdgcn_raw_buffer_store_b64(tag, rws, f1o, 0, SC1);
    if constexpr (s == 4 && n == 6) { o_u = OFF_XT + (idx >> 2) * 32768 + (w * 32 + (lane >> 1)) * 128 + (idx & 3) * 32 + (lane & 1) * 16; o_fx = OFF_F1 + (w * 32 + r32) * 8; PIN(o_u); PIN(o_fx); }
    if constexpr (s == 6 && n == 9) ALDW64(fx2, o_fx);
    if constexpr (s == 7 && n == 6) { PIN(fx2); early = wall(TAGOK(fx2)); ALDW128(uv0, early ? o_u : OFF_F1, 0); }
    if constexpr (s == 6 && n == 2) aldw64o<0>(cg[0], o_cs, ws);
    if constexpr (s == 6 && n == 6) aldw64o<1536>(cg[1], o_cs, ws);
    if constexpr (s == 6 && n == 7) aldw64o<3072>(cg[2], o_cs, ws);
    if constexpr (s == 6 && n == 8) aldw64o<-1536>(cg[3], o_cs, ws);
    if constexpr (s == 6 && n == 9) ALDW64(sg, o_st);
    if constexpr (s == 7 && n == 5) { wait5(sg, cg); cp0 = csred0(); PIN(cp0); }
    if constexpr (s == 7 && n == 6) { cp1 = csred1(cp0); PIN(cp1); }
    if constexpr (s == 7 && n == 7) csred2(cp1);
    if constexpr (s == 7 && n == 8) { ctag = cstag(); PIN(ctag); }
    if constexpr (s == 7 && n == 9) {
      if (__builtin_expect(!wall(ctag == 0u), 0)) {
#pragma unroll 1
        for (int i = 0; i < SPIN_G && !wall(cstag() == 0u); i++) { __builtin_amdgcn_s_sleep(8); ldcs(); wait0(sg, cg); }
        csred2(csred1(csred0()));
      }
    }
  };
  auto stage = [&](auto sc) {
    constexpr int s = decltype(sc)::value, SP = ((s + 2) % 3) * STG;
    auto dma = [&](auto ic) {
      constexpr int i = decltype(ic)::value;
      if constexpr (s >= 6) dma16<SP + i * 2048>(ld, wdo, rd, kso((s - 6) * 10 + i, fh * 5));
      else if constexpr (i < 3) dma16<SP + i * 1024>(lw, wo, i ? rw2 : rw, sow(s + 2) + i * 64);
      else dma16<SP + (i - 3) * 1024>(lx, i == 3 ? xo : xo2, rx, sox(s + 2));
    };
    auto slot = [&](auto nc) {
      constexpr int n = decltype(nc)::value, k = n / 3, g = n % 3;
      acc[g] = MFMA32(W[g], X, acc[g]);
      if constexpr (s == 0) cs[g] = MFMA16(csel, W[g], cs[g]);
      if constexpr (k < 3) W[g] = rdw(s, k + 1, g);
      if constexpr (n == 0) dma(IC<0>{});
      if constexpr (n == 1) dma(IC<1>{});
      if constexpr (n == 3) dma(IC<2>{});
      if constexpr (n == 4) dma(IC<3>{});
      if constexpr (n == 5) dma(IC<4>{});
      SCHED();
      side(sc, nc);
      SCHED();
    };
    bf16x8 X1 = rdx(s, 1);
    slot(IC<0>{}); slot(IC<1>{}); slot(IC<2>{});
    X = X1; X1 = rdx(s, 2);
    slot(IC<3>{}); slot(IC<4>{}); slot(IC<5>{});
    X = X1; X1 = rdx(s, 3);
    slot(IC<6>{}); slot(IC<7>{}); slot(IC<8>{});
    X = X1;
    slot(IC<9>{});
    GST(s);
    if (s == 2) VBAR(7);
    else if (s <= 3) VBAR(6);
    else if (s == 6) VBAR(11);
    else if (s == 7) FBAR();
    else VBAR(5);
    if (s < 7) { X1 = rdx(s + 1, 0); W[0] = rdw(s + 1, 0, 0); }
    SCHED();
#pragma unroll
    for (int g = 1; g < 3; g++) { acc[g] = MFMA32(W[g], X, acc[g]); if (s == 0) cs[g] = MFMA16(csel, W[g], cs[g]); if (s < 7) W[g] = rdw(s + 1, 0, g); SCHED(); }
    X = X1;
  };
  stage(IC<0>{}); stage(IC<1>{}); stage(IC<2>{}); stage(IC<3>{}); stage(IC<4>{}); stage(IC<5>{}); stage(IC<6>{}); stage(IC<7>{});
  PST(4); GST(8);
  __builtin_amdgcn_s_setprio(2);
  {
    const u32 stt = sg[0];
    const u32 nm = h ? 0u : (((stt & 0xffffu) ^ 0x8000u) * 0x10001u);
    const bf16x8 mb = __builtin_bit_cast(bf16x8, u32x4{nm, 0u, 0u, 0u});
#pragma unroll
    for (int g = 0; g < 3; g++) acc[g] = MFMA32(__builtin_bit_cast(bf16x8, u32x4{*(const LAS u32*)(csr + g * 128), 0u, 0u, 0u}), mb, acc[g]);
    const float sd = bfhi(stt), c1 = -1.44269504088896341f * __builtin_amdgcn_rcpf(sd), s2 = sd * sd;
#pragma unroll
    for (int g = 0; g < 3; g++) {
      u32x4 o;
#pragma unroll
      for (int r = 0; r < 4; r++) {
        const f32x2 gg = {acc[g][2 * r], acc[g][2 * r + 1]}, u = {acc[g][2 * r + 8], acc[g][2 * r + 9]};
        const f32x2 t = gg * f32x2{c1, c1};
        const f32x2 e = {__builtin_amdgcn_exp2f(t[0]), __builtin_amdgcn_exp2f(t[1])};
        const f32x2 dn = __builtin_elementwise_fma(e, f32x2{s2, s2}, f32x2{s2, s2});
        o[r] = pkc(gg * u * f32x2{__builtin_amdgcn_rcpf(dn[0]), __builtin_amdgcn_rcpf(dn[1])});
      }
      __builtin_amdgcn_raw_buffer_store_b128(o, rws, grp * 6 + 3 * fh + g < KS2 ? ho + g * 1024 : OOB, 0, SC1);
    }
  }
  SCHED();
  __builtin_amdgcn_s_setprio(0);
  PST(5);
  asm volatile("s_waitcnt vmcnt(3)" : "+v"(uv0) :: "memory");
  if (__builtin_expect(!early, 0)) {
#pragma unroll 1
    for (int i = 0; i < SPIN_G && !wall(TAGOK(fx2)); i++) { __builtin_amdgcn_s_sleep(4); ALDW64(fx2, o_fx); asm volatile("s_waitcnt vmcnt(0)" : "+v"(fx2) :: "memory"); }
    ALDW128(uv0, o_u, 0);
    asm volatile("s_waitcnt vmcnt(0)" : "+v"(uv0) :: "memory");
  }
  {
    *(LAS u32x4*)(ud + CV_UA + 512) = uv0;
#pragma unroll
    for (int e = 0; e < 4; e++) *(LAS u32*)(ud + CV_UB + 516 + e * 4) = uv0[e];
  }
  auto aoff = [&](int i) { int k = i >= 30 ? i - 30 : i; return ((k / 10 + 2) % 3) * STG + (k % 10) * 2048; };
  if (w >= 4) {
    lds_post(pme, 1);
    const u32 l2 = (u32)(size_t)sm + STG + kh * 20480 + th * 1024;
    ra_[0] = (u32)(size_t)sm + kh * 20480 + (4 * h + q4) * 128 + (((q & 1) * 32 + (lane & 3) * 8) ^ (((q4 >> 1) ^ kh) * 64));
    ra_[1] = ra_[0] ^ 64; ra_[2] = ra_[0] + 2 * STG; ra_[3] = ra_[1] + 2 * STG;
    ra_[4] = (u32)(size_t)sm + kh * 20480 + th * 1024 + lane * 16; ra_[5] = ra_[4] + 2 * STG;
    ra_[6] = OFF_HF + (T * KS2 + kh * 43 + rot - 43) * 1024 + lane * 16;
    ra_[7] = OFF_F2 + (T * 32 + kh * 14 + c16) * 128;
    ra_[8] = (u32)(size_t)sm + STG + kh * 20480 + th * 4096 + lane * 16;
    ra_[9] = (u32)(size_t)sm + STG + (kh ^ 1) * 20480 + th * 4096 + lane * 16;
    ra_[10] = (u32)(((T * 32 + r32) * D + dg * 64 + kh * 32 + h * 8) * 2);
    ra_[12] = (u32)(size_t)cc + CV_RA + th * 4096 + lane * 16;
#pragma unroll
    for (int k = 0; k < 13; k++) if (k != 11) PIN(ra_[k]);
    WAITV(0);
    __builtin_amdgcn_raw_buffer_store_b64(tag, rws, f2p, 0, SC1);
    PST(6);
    dma16<0>(l2, wdo, rd, kso(20, 0)); dma16<2048>(l2, wdo, rd, kso(21, 0)); dma16<4096>(l2, wdo, rd, kso(22, 0)); dma16<6144>(l2, wdo, rd, kso(23, 0));
    dma16<8192>(l2, wdo, rd, kso(24, 0)); dma16<10240>(l2, wdo, rd, kso(25, 0)); dma16<12288>(l2, wdo, rd, kso(26, 0));
    dma16<14336>(l2, wdo, rd, kso(27, 0)); dma16<16384>(l2, wdo, rd, kso(28, 0)); dma16<18432>(l2, wdo, rd, kso(29, 0));
    u32x4 wpark[13];
    auto rdd = __builtin_amdgcn_make_buffer_rsrc((void*)wd, 0, F * D * 2, 0x00020000);
#pragma unroll
    for (int k = 0; k < 13; k++) wpark[k] = __builtin_amdgcn_raw_buffer_load_b128(rdd, wdo, kso(30 + k, 0), 0);
    PST(7);
    const LAS char* ard[2][2] = {{LP(0), LP(1)}, {LP(2), LP(3)}};
    LAS char* pk[2] = {LP(4), LP(5)};
    const u32 hvo = ra_[6];
    auto rda = [&](int i, int dt) { const int o = aoff(i); return o >= 2 * STG ? trfrag<1024>(ard[1][dt] + (o - 2 * STG)) : trfrag<1024>(ard[0][dt] + o); };
    lds_wait(pall, 1);
    bf16x8 A[3][2] = {{rda(0, 0), rda(0, 1)}, {rda(1, 0), rda(1, 1)}, {}};
    const u32 f2o = ra_[7];
    u32x2 f2 = {0, 0};
    __builtin_amdgcn_s_sleep(PDELAY);
#pragma unroll 1
    for (int i = 0; i < SPIN_G; i++) {
      ALDW64(f2, f2o);
      asm volatile("s_waitcnt vmcnt(0)" : "+v"(f2) :: "memory");
      if (wall(TAGOK(f2))) break;
    }
    PST(8);
    constexpr int HP = 12;
    u32x4 Hf[HP];
    auto ldh = [&](int i) { return __builtin_amdgcn_raw_buffer_load_b128(rws, hvo + (u32)(i & 3) * 1024u, (u32)((i & ~3) * 1024) + (i >= wrap ? 0u : 43u * 1024u), SC1); };
#pragma unroll
    for (int i = 0; i < HP; i++) Hf[i] = ldh(i);
    f32x16 a0 = {}, a1 = {};
    u32 pg = 0;
#pragma unroll
    for (int i = 0; i < 43; i++) {
      if (i == 13 && __builtin_expect((u32)__builtin_amdgcn_readfirstlane(pg) < 2u, 0)) lds_wait(ppar, 2);
      if (i == 28 && __builtin_expect((u32)__builtin_amdgcn_readfirstlane(pg) < 3u, 0)) lds_wait(ppar, 3);
      if (i == 13 || i == 28) asm volatile("" ::: "memory");
      if (i + 2 < 43) { A[(i + 2) % 3][0] = rda(i + 2, 0); A[(i + 2) % 3][1] = rda(i + 2, 1); }
      a0 = MFMA32(A[i % 3][0], __builtin_bit_cast(bf16x8, Hf[i % HP]), a0);
      a1 = MFMA32(A[i % 3][1], __builtin_bit_cast(bf16x8, Hf[i % HP]), a1);
      if (i + HP < 43) Hf[i % HP] = ldh(i + HP);
      if (i == 10) lds_post(pme, 2);
      if (i == 12 || i == 27) pg = *(const volatile LAS u32*)ppar;
      if (i >= 13 && i < 24) { const int o = aoff(i - 13); *(LAS u32x4*)(o >= 2 * STG ? pk[1] + (o - 2 * STG) : pk[0] + o) = wpark[i - 13]; }
      if (i == 22 || i == 23) *(LAS u32x4*)(pk[0] + aoff(i - 11)) = wpark[i - 11];
      if (i == 24) lds_post(pme, 3);
      SCHED();
    }
    PST(9);
    {
      LAS char* rb = LP(8);
#pragma unroll
      for (int g = 0; g < 4; g++) *(LAS f32x4*)(rb + g * 1024) = f32x4{a1[4 * g], a1[4 * g + 1], a1[4 * g + 2], a1[4 * g + 3]};
    }
    const f32x16 kp = a0;
    const LAS char* rb = LP(9);
    const LAS char* rr = LP(12);
    char* op = (char*)out + ra_[10];
    FBAR();
    PST(10);
    u32x2 o[4];
#pragma unroll
    for (int g = 0; g < 4; g++) {
      const f32x4 v = (*(const LAS f32x4*)(rb + g * 1024) + f32x4{kp[4 * g], kp[4 * g + 1], kp[4 * g + 2], kp[4 * g + 3]}) + *(const LAS f32x4*)(rr + g * 1024);
      o[g] = u32x2{pk2(v[0], v[1]), pk2(v[2], v[3])};
    }
#pragma unroll
    for (int g = 0; g < 4; g += 2) {
      auto s0 = __builtin_amdgcn_permlane32_swap(o[g][0], o[g + 1][0], false, false);
      auto s1 = __builtin_amdgcn_permlane32_swap(o[g][1], o[g + 1][1], false, false);
      *(u32x4*)(op + g * 16) = u32x4{s0[0], s1[0], s0[1], s1[1]};
    }
  } else {
    {
      const u32 e0 = *(const LAS u16*)LP(1), e1 = *(const LAS u16*)LP(2), en = *(const LAS u16*)LP(5);
      const u32x2 ea = *(const LAS u32x2*)LP(3), ec = *(const LAS u32x2*)(LP(0) + 8);
      u32 d0 = *(const LAS u32*)(LP(4) + 12);
      LAS char* ra = LP(6) + CV_RA;
      const u32 SW = 0x01000302u, HL = 0x03020504u, HH = 0x03020302u;
      const u32 q0 = PERM(e0, e0), q1 = PERM(e1, e1), q10 = PERM(e0, e1);
      *(LAS u32x4*)(ra - 2048) = u32x4{q1, (lane & 1) ? q10 : q0, q0, q0};
      *(LAS u32x4*)(ra - 2048 + (CV_RB - CV_RA)) = u32x4{q1, (lane & 1) ? q1 : q10, q0, q0};
      WAITV(0);
      __builtin_amdgcn_raw_buffer_store_b64(tag, rws, f2p, 0, SC1);
      u32x4 va, vb;
      if (th) {
        const u32 a0 = ea[0], a1 = ea[1], d1 = h ? a1 : a0, p0 = PERM(d1, d1), p1 = __builtin_amdgcn_perm(d1, d1, HH);
        va = u32x4{__builtin_amdgcn_perm(en, d1, HL), p1, p0, p0}; vb = u32x4{PERM(en, en), p1, __builtin_amdgcn_perm(d1, d1, SW), p0};
      } else {
        d0 = lane == 0 ? 0u : d0;
        const u32 d1 = ea[0], d2 = ea[1], d3 = ec[0], d4 = ec[1], p0 = PERM(d1, d1), p2 = PERM(d2, d2);
        const u32 s1 = __builtin_amdgcn_perm(d1, d1, SW), s2 = __builtin_amdgcn_perm(d2, d2, SW), t10 = __builtin_amdgcn_perm(d1, d0, HL), t21 = __builtin_amdgcn_perm(d2, d1, HL);
        va = h ? u32x4{s2, p2, s1, lane == 32 ? t10 : p0} : u32x4{__builtin_amdgcn_perm(d4, d3, HL), __builtin_amdgcn_perm(d3, d2, HL), t21, t10};
        vb = h ? u32x4{__builtin_amdgcn_perm(en, d2, HL), p2, t21, p0} : u32x4{__builtin_amdgcn_perm(d4, d4, SW), __builtin_amdgcn_perm(d3, d3, SW), s2, s1};
      }
      *(LAS u32x4*)ra = va; *(LAS u32x4*)(ra + (CV_RB - CV_RA)) = vb;
      LAS char* z = LP(7);
      *(LAS u32*)(z + th * (CV_UB + 4)) = 0u; *(LAS u32*)(z + th * (CV_UB + 4) + 4) = 0u;
      if (lane < 8) *(LAS u32x2*)(z + CV_RA + 4096 + th * (CV_RB - CV_RA)) = u32x2{0, 0};
    }
    lds_post(pme, 1);
    u32x4 x0, x1;
    ALD128(x0, x, ra_[11]); ALD128(x1, x + 16 * D, ra_[11]);
    const LAS char* ub = LP(8); const LAS char* tbp = LP(9);
    const u32 yso = ra_[10];
    lds_wait(pall, 1);
    PST(6);
    f32x4 ya[8] = {};
    conv_main(ub, tbp, ya, [&](int m) { if (m == 108) { WAITV(0); lds_post(pme, 2); } });
    PST(7);
    lds_wait(ppar, 2);
#pragma unroll
    for (int a = 0; a < 4; a++) {
      f32x4 lo = ya[a], hi = ya[4 + a];
      asm volatile("" : "+v"(lo), "+v"(hi));
      *(LAS f32x4*)(LP(12) + (th ? -4096 : 4096) + a * 1024) = th ? lo : hi;
      ya[a] = th ? hi : lo;
    }
    lds_post(pme, 3);
    lds_wait(ppar, 3);
    u32 p[4][2];
#pragma unroll
    for (int a = 0; a < 4; a++) {
      const f32x4 yv = ya[a] + *(const LAS f32x4*)(LP(12) + a * 1024);
#pragma unroll
      for (int e = 0; e < 2; e++) { const u32 pv = pk2(yv[2 * e], yv[2 * e + 1]); p[a][e] = pv < 0xfffffffeu ? pv : 0xfffffffeu; }
    }
#pragma unroll
    for (int a = 0; a < 4; a += 2) {
      const bool odd = c16 & 1;
      u32 s0 = odd ? p[a][0] : p[a + 1][0], s1 = odd ? p[a][1] : p[a + 1][1];
      u32 r0 = __builtin_amdgcn_mov_dpp(s0, 0xB1, 0xf, 0xf, true), r1 = __builtin_amdgcn_mov_dpp(s1, 0xB1, 0xf, 0xf, true);
      u32x4 o = {odd ? r0 : p[a][0], odd ? p[a + 1][0] : r0, odd ? r1 : p[a][1], odd ? p[a + 1][1] : r1};
      __builtin_amdgcn_raw_buffer_store_b128(o, rws, yso + a * 512, 0, SC1);
    }
    PST(8);
    if (b == 0 && tid == 0) __builtin_amdgcn_raw_buffer_store_b32(ep, rws, 0, OFF_EP, SC1);
    asm volatile("" : "+v"(x0), "+v"(x1));
    *(LAS u32x4*)(cc + 4096 + th * 2560 + (lane >> 2) * 80 + (lane & 3) * 16) = x0;
    *(LAS u32x4*)(cc + 4096 + th * 2560 + (16 + (lane >> 2)) * 80 + (lane & 3) * 16) = x1;
    f32x4 xf[4];
#pragma unroll
    for (int g = 0; g < 4; g++) {
      const u32x2 xr = *(const LAS u32x2*)(cc + 4096 + th * 2560 + r32 * 80 + h * 8 + g * 16);
      const u32 xa0 = xr[0], xa1 = xr[1];
      xf[g] = f32x4{bflo(xa0), bfhi(xa0), bflo(xa1), bfhi(xa1)};
      PIN(xf[g]);
    }
    const LAS char* yb = cc + th * 2048 + (4 * h + q4) * 64 + (q & 1) * 32 + (lane & 3) * 8;
    LAS char* yw = cc + th * 2048 + lane * 16;
    PIN(yb); PIN(yw);
    lds_wait(pme + 16, YGATE);
    const u32 yo = OFF_YT + ((dg * 64 + kh * 32 + (lane >> 2)) * L + T * 32 + (lane & 3) * 8) * 2;
    u32x4 y0, y1;
#pragma unroll 1
    for (int i = 0; i < SPIN_G; i++) {
      y0 = __builtin_amdgcn_raw_buffer_load_b128(rws, yo, 0, SC1); y1 = __builtin_amdgcn_raw_buffer_load_b128(rws, yo, 16 * L * 2, SC1);
      const u32x4 m = __builtin_elementwise_max(y0, y1);
      const u32 m0 = m[0] > m[1] ? m[0] : m[1], m1 = m[2] > m[3] ? m[2] : m[3];
      if (wall((m0 > m1 ? m0 : m1) != ~0u)) break;
      __builtin_amdgcn_s_sleep(8);
      asm volatile("" ::: "memory");
    }
    *(LAS u32x4*)yw = y0;
    *(LAS u32x4*)(yw + 1024) = y1;
#pragma unroll
    for (int g = 0; g < 4; g++) {
      const u32x2 y = trread(yb + g * 512);
      const u32 ya0 = y[0], ya1 = y[1];
      *(LAS f32x4*)(LP(12) + g * 1024) = xf[g] + f32x4{bflo(ya0), bfhi(ya0), bflo(ya1), bfhi(ya1)};
    }
    PST(9);
    FBAR();
  }
}

extern "C" void kernel_launch(void* const* d_in, const int* in_sizes, int n_in, void* d_out, int out_size, void* d_ws, size_t ws_size, hipStream_t stream) {
  const u16* const* in = (const u16* const*)d_in;
  k_fused<<<256, 512, 0, stream>>>(in[0], in[1], in[2], in[3], in[4], in[5], in[6], in[7], in[8], in[10], in[11], in[12], (char*)d_ws, (u16*)d_out);
}
```
